```python
import torch

def _softmax(x, axis=-1):
    return torch.softmax(x, dim=axis)
def _gelu(x):
    return torch.nn.functional.gelu(x, approximate="tanh")
def _silu(x):
    return torch.nn.functional.silu(x)
def _sigmoid(x):
    return torch.sigmoid(x)
def _layernorm(x, axis=-1, eps=1e-5):
    m = x.mean(dim=axis, keepdim=True)
    v = x.var(dim=axis, keepdim=True, unbiased=False)
    return (x - m) / torch.sqrt(v + eps)
def _scatter_add(tgt, idx, src, axis=0):
    return tgt.clone().index_add_(axis, idx, src)
def _segment_sum(src, seg, n):
    out = torch.zeros((n,) + src.shape[1:], dtype=src.dtype)
    return out.index_add_(0, seg, src)
def _pad(x, pw):
    flat = tuple(v for pair in reversed(pw) for v in pair)
    return torch.nn.functional.pad(x, flat)

kernel_name = 'sequential_preLN__sliding_block_cached__dense_activation_mish__decode'

def setup_inputs():

    _w = (lambda a: (lambda t: t.to(torch.bfloat16) if t.is_floating_point() else t)(torch.as_tensor(a)))
    def _A_setup():
        B, Sq, D = 1, 1, 448
        Hq, Hkv, Sk, Dh = 16, 8, 8192, 256
        assert Hq % Hkv == 0, (Hq, Hkv)
        return {
            "x": _w((lambda *s: torch.randn(*s, dtype=torch.bfloat16) * 0.05)(B, Sq, D)),
            "k_cache": _w((lambda *s: torch.randn(*s, dtype=torch.bfloat16) * 1.6)(B, Hkv, Sk, Dh)),
            "v_cache": _w((lambda *s: torch.randn(*s, dtype=torch.bfloat16) * 0.4)(B, Hkv, Sk, Dh)),
            "wq": _w((lambda *s: torch.randn(*s, dtype=torch.bfloat16) * 1.6)(D, Hq * Dh) * (D ** -0.5)),
            "wo": _w((lambda *s: torch.randn(*s, dtype=torch.bfloat16) * 0.4)(Hq * Dh, D) * ((Hq * Dh) ** -0.5)),
        }
    _A_in = _A_setup()

    def _B_setup():
        B, S, D, F = 1, 1, 448, 1792
        return {
            "x": _w((lambda *s: torch.randn(*s, dtype=torch.bfloat16) * 0.05)(B, S, D)),
            "w1": _w((lambda *s: torch.randn(*s, dtype=torch.bfloat16) * 0.8)(D, F) * (D ** -0.5)),
            "b1": _w((lambda *s: torch.randn(*s, dtype=torch.bfloat16) * 0.2)(F)),
            "w2": _w((lambda *s: torch.randn(*s, dtype=torch.bfloat16) * 0.1)(F, D) * (F ** -0.5)),
        }
    _B_in = _B_setup()

    return {
        "A_x": _A_in["x"],
        "A_k_cache": _A_in["k_cache"],
        "A_v_cache": _A_in["v_cache"],
        "A_wq": _A_in["wq"],
        "A_wo": _A_in["wo"],
        "B_x": _B_in["x"],
        "B_w1": _B_in["w1"],
        "B_b1": _B_in["b1"],
        "B_w2": _B_in["w2"],
    }

def reference(A_x, A_k_cache, A_v_cache, A_wq, A_wo, B_x, B_w1, B_b1, B_w2):
    def _A_ref(x, k_cache, v_cache, wq, wo):
        B, Sq, D = x.shape
        Hq, Dh = 16, 256
        Hkv, Sk = k_cache.shape[1], k_cache.shape[2]
        W = min(4096, max(256, Sk // 4))
        rep = Hq // Hkv
        q = (lambda x, *d: x.permute(*d))((x @ wq).reshape(B, Sq, Hq, Dh), 0, 2, 1, 3)
        k = (lambda x, n, axis: torch.repeat_interleave(x, n, dim=axis))(k_cache[:, :, Sk - W:, :], rep, axis=1)
        v = (lambda x, n, axis: torch.repeat_interleave(x, n, dim=axis))(v_cache[:, :, Sk - W:, :], rep, axis=1)
        scores = torch.einsum("bhqd,bhkd->bhqk", q, k) * (Dh ** -0.5)
        p = _softmax(scores, axis=-1)
        o = torch.einsum("bhqk,bhkd->bhqd", p, v)
        return (lambda x, *d: x.permute(*d))(o, 0, 2, 1, 3).reshape(B, Sq, Hq * Dh) @ wo

    def _B_ref(x, w1, b1, w2):
        h = torch.einsum("bsd,df->bsf", x, w1) + b1
        sp = torch.log1p(torch.exp(-torch.abs(h))) + torch.where(h > 0, h, torch.zeros_like(h))
        act = h * torch.tanh(sp)
        return torch.einsum("bsf,fd->bsd", act, w2)

    x = A_x
    h = x + _A_ref(_layernorm(x, axis=-1), A_k_cache, A_v_cache, A_wq, A_wo)
    h2 = h + _B_ref(_layernorm(h, axis=-1), B_w1, B_b1, B_w2)
    return h2
```

```cpp
#include <hip/hip_runtime.h>
#include <hip/hip_bf16.h>
#include <stdint.h>

typedef unsigned short u16;
__device__ __forceinline__ float bf2f(u16 v) { return __uint_as_float(((unsigned)v) << 16); }
__device__ __forceinline__ float rbf(float f) {
  unsigned u = __float_as_uint(f); u = (u + 0x7FFFu + ((u >> 16) & 1u)) & 0xFFFF0000u; return __uint_as_float(u); }
__device__ __forceinline__ u16 f2bf(float f) { unsigned u = __float_as_uint(f); u = (u + 0x7FFFu + ((u >> 16) & 1u)); return (u16)(u >> 16); }

#define D 448
#define HQ 16
#define HKV 8
#define SK 8192
#define DH 256
#define W 2048
#define F 1792

__device__ void ln448(const float* xin, float* xo, float* red) {
  int t = threadIdx.x;
  float s = 0.f;
  for (int i = t; i < D; i += blockDim.x) s += xin[i];
  red[t] = s; __syncthreads();
  for (int o = blockDim.x / 2; o > 0; o >>= 1) { if (t < o) red[t] += red[t + o]; __syncthreads(); }
  float mean = red[0] / D; __syncthreads();
  s = 0.f;
  for (int i = t; i < D; i += blockDim.x) { float d = xin[i] - mean; s += d * d; }
  red[t] = s; __syncthreads();
  for (int o = blockDim.x / 2; o > 0; o >>= 1) { if (t < o) red[t] += red[t + o]; __syncthreads(); }
  float var = red[0] / D; __syncthreads();
  float r = rsqrtf(var + 1e-5f);
  for (int i = t; i < D; i += blockDim.x) xo[i] = rbf((xin[i] - mean) * r);
  __syncthreads();
}

__global__ void k_q(const u16* x, const u16* wq, float* q) {
  __shared__ float xs[D], xn[D], red[256];
  for (int i = threadIdx.x; i < D; i += 256) xs[i] = bf2f(x[i]);
  __syncthreads();
  ln448(xs, xn, red);
  int c = blockIdx.x * 256 + threadIdx.x;
  float acc = 0.f;
  for (int k = 0; k < D; k++) acc += xn[k] * bf2f(wq[(size_t)k * 4096 + c]);
  q[c] = rbf(acc);
}
__global__ void k_attn(const float* q, const u16* K, const u16* V, float* o) {
  __shared__ float sc[W], qs[DH], red[256];
  int h = blockIdx.x, g = h / 2, t = threadIdx.x;
  qs[t] = q[h * DH + t]; __syncthreads();
  const u16* Kg = K + ((size_t)g * SK + (SK - W)) * DH;
  const u16* Vg = V + ((size_t)g * SK + (SK - W)) * DH;
  float m = -1e30f;
  for (int s = t; s < W; s += 256) {
    float a = 0.f;
    for (int d = 0; d < DH; d++) a += qs[d] * bf2f(Kg[(size_t)s * DH + d]);
    a = rbf(a) * 0.0625f; sc[s] = a; m = fmaxf(m, a);
  }
  red[t] = m; __syncthreads();
  for (int o2 = 128; o2 > 0; o2 >>= 1) { if (t < o2) red[t] = fmaxf(red[t], red[t + o2]); __syncthreads(); }
  m = red[0]; __syncthreads();
  float l = 0.f;
  for (int s = t; s < W; s += 256) { float e = __expf(sc[s] - m); sc[s] = e; l += e; }
  red[t] = l; __syncthreads();
  for (int o2 = 128; o2 > 0; o2 >>= 1) { if (t < o2) red[t] += red[t + o2]; __syncthreads(); }
  l = red[0];
  float a = 0.f;
  for (int s = 0; s < W; s++) a += rbf(sc[s] / l) * bf2f(Vg[(size_t)s * DH + t]);
  o[h * DH + t] = rbf(a);
}
__global__ void k_o(const float* o, const u16* wo, const u16* x, float* h) {
  __shared__ float red[256];
  int c = blockIdx.x * 64 + (threadIdx.x & 63), kg = threadIdx.x >> 6;
  float a = 0.f;
  for (int k = kg * 1024; k < (kg + 1) * 1024; k++) a += o[k] * bf2f(wo[(size_t)k * D + c]);
  red[threadIdx.x] = a; __syncthreads();
  if (kg == 0) { a = red[threadIdx.x] + red[threadIdx.x + 64] + red[threadIdx.x + 128] + red[threadIdx.x + 192];
    h[c] = rbf(bf2f(x[c]) + rbf(a)); }
}
__global__ void k_f1(const float* h, const u16* w1, const u16* b1, float* act) {
  __shared__ float xs[D], xn[D], red[256];
  for (int i = threadIdx.x; i < D; i += 256) xs[i] = h[i];
  __syncthreads();
  ln448(xs, xn, red);
  int c = blockIdx.x * 256 + threadIdx.x;
  float acc = 0.f;
  for (int k = 0; k < D; k++) acc += xn[k] * bf2f(w1[(size_t)k * F + c]);
  float u = rbf(rbf(acc) + bf2f(b1[c]));
  float sp = log1pf(expf(-fabsf(u))) + fmaxf(u, 0.f);
  act[c] = rbf(u * tanhf(sp));
}
__global__ void k_f2(const float* act, const u16* w2, const float* h, u16* out) {
  __shared__ float red[256];
  int c = blockIdx.x * 64 + (threadIdx.x & 63), kg = threadIdx.x >> 6;
  float a = 0.f;
  for (int k = kg * 448; k < (kg + 1) * 448; k++) a += act[k] * bf2f(w2[(size_t)k * D + c]);
  red[threadIdx.x] = a; __syncthreads();
  if (kg == 0) { a = red[threadIdx.x] + red[threadIdx.x + 64] + red[threadIdx.x + 128] + red[threadIdx.x + 192];
    out[c] = f2bf(h[c] + rbf(a)); }
}

extern "C" void kernel_launch(void* const* d_in, const int* in_sizes, int n_in,
                              void* d_out, int out_size, void* d_ws, size_t ws_size,
                              hipStream_t stream) {
  const u16* x = (const u16*)d_in[0];
  const u16* K = (const u16*)d_in[1];
  const u16* V = (const u16*)d_in[2];
  const u16* wq = (const u16*)d_in[3];
  const u16* wo = (const u16*)d_in[4];
  const u16* w1 = (const u16*)d_in[6];
  const u16* b1 = (const u16*)d_in[7];
  const u16* w2 = (const u16*)d_in[8];
  float* ws = (float*)d_ws;
  float* q = ws; float* o = ws + 4096; float* h = ws + 8192; float* act = ws + 8192 + 512;
  k_q<<<16, 256, 0, stream>>>(x, wq, q);
  k_attn<<<16, 256, 0, stream>>>(q, K, V, o);
  k_o<<<7, 256, 0, stream>>>(o, wo, x, h);
  k_f1<<<7, 256, 0, stream>>>(h, w1, b1, act);
  k_f2<<<7, 256, 0, stream>>>(act, w2, h, (u16*)d_out);
}
```

```cpp
#include <hip/hip_runtime.h>
#include <stdint.h>

#define GAS __attribute__((address_space(1)))
#define LAS __attribute__((address_space(3)))
typedef unsigned u32;
typedef unsigned short u16;
typedef u32 v4u __attribute__((ext_vector_type(4)));
typedef u32 v2u __attribute__((ext_vector_type(2)));
typedef float f32x4 __attribute__((ext_vector_type(4)));
typedef float f32x2 __attribute__((ext_vector_type(2)));
typedef short bf16x8 __attribute__((ext_vector_type(8)));
typedef __bf16 bf16x2 __attribute__((ext_vector_type(2)));

#define NT_LOADS 1
#define NT_WQ 0
#define NT_TAIL 0
#define TAIL_WAIT_Q 1
#define QPOLL_AT 4
#define QDELAY 0
#define CLRW 3
#define QSLEEP 0
#define STAMPS 0

constexpr int D = 448, DH = 256, SK = 8192, WIN = 2048, F = 1792, NQ = 4096;
constexpr int NWG = 256, NTHR = 512;
constexpr int AUX_SC1 = 16;
constexpr u32 SENT = 0xFFFFFFFFu;
constexpr u32 SPIN_CAP = 1u << 18;
constexpr int EPOCH_OFF = 0;
constexpr int RDY_OFF = 1024;
constexpr int Q1_OFF = 4096;
constexpr int S2_OFF = Q1_OFF + 65536;
constexpr int P2_OFF = 81920;
constexpr int P3_OFF = P2_OFF + 256 * 2048;
constexpr int P4_OFF = P3_OFF + 8 * 16 * 1024;
constexpr int WS_BYTES = P4_OFF + 112 * 2048;
constexpr int STAMP_OFF = 2 << 20;

__device__ __forceinline__ float asf(u32 u) { return __builtin_bit_cast(float, u); }
__device__ __forceinline__ u32 asu(float f) { return __builtin_bit_cast(u32, f); }
__device__ __forceinline__ float bflo(u32 w) { return asf(w << 16); }
__device__ __forceinline__ float bfhi(u32 w) { return asf(w & 0xFFFF0000u); }
__device__ __forceinline__ u32 pkbf(float lo, float hi) {
  bf16x2 r; r.x = (__bf16)lo; r.y = (__bf16)hi; return __builtin_bit_cast(u32, r);
}
__device__ __forceinline__ u32 nosent(u32 v) { return v < 0xFFFFFFFEu ? v : 0xFFFFFFFEu; }
template <int CTRL> __device__ __forceinline__ float dpp(float x) {
  return asf(__builtin_amdgcn_update_dpp(0, asu(x), CTRL, 0xF, 0xF, true));
}
#define DPP_XOR1 0xB1
#define DPP_XOR2 0x4E
#define DPP_ROR4 0x124
#define DPP_ROR8 0x128
__device__ __forceinline__ float add_x16(float x) {
  auto r = __builtin_amdgcn_permlane16_swap(asu(x), asu(x), false, false);
  return asf(r[0]) + asf(r[1]);
}
__device__ __forceinline__ float add_x32(float x) {
  auto r = __builtin_amdgcn_permlane32_swap(asu(x), asu(x), false, false);
  return asf(r[0]) + asf(r[1]);
}
__device__ __forceinline__ float max_x16(float x) {
  auto r = __builtin_amdgcn_permlane16_swap(asu(x), asu(x), false, false);
  return fmaxf(asf(r[0]), asf(r[1]));
}
__device__ __forceinline__ float max_x32(float x) {
  auto r = __builtin_amdgcn_permlane32_swap(asu(x), asu(x), false, false);
  return fmaxf(asf(r[0]), asf(r[1]));
}
__device__ __forceinline__ float row_sum16(float x) {
  x += dpp<DPP_XOR1>(x); x += dpp<DPP_XOR2>(x); x += dpp<DPP_ROR4>(x); x += dpp<DPP_ROR8>(x); return x;
}
__device__ __forceinline__ float wave_sum(float x) { return add_x32(add_x16(row_sum16(x))); }
__device__ __forceinline__ f32x4 mfma(v4u a, v4u b, f32x4 c) {
  return __builtin_amdgcn_mfma_f32_16x16x32_bf16(__builtin_bit_cast(bf16x8, a), __builtin_bit_cast(bf16x8, b), c, 0, 0, 0);
}
__device__ __forceinline__ float dot2(u32 a, u32 b, float c) {
  return __builtin_amdgcn_fdot2_f32_bf16(__builtin_bit_cast(bf16x2, a), __builtin_bit_cast(bf16x2, b), c, false);
}
__device__ __forceinline__ u32 plo(u32 hi, u32 lo) { return __builtin_amdgcn_perm(hi, lo, 0x05040100u); }
__device__ __forceinline__ u32 phi(u32 hi, u32 lo) { return __builtin_amdgcn_perm(hi, lo, 0x07060302u); }
template <typename T> __device__ __forceinline__ T ldg(const void* p) {
  return __builtin_nontemporal_load((const GAS T*)p);
}
template <typename T, int NT> __device__ __forceinline__ T ldx(const void* p) {
  if (NT) return __builtin_nontemporal_load((const GAS T*)p); else return *(const GAS T*)p;
}
template <typename T> __device__ __forceinline__ T ldp(const void* p) { return *(const GAS T*)p; }
#define BAR() do { __builtin_amdgcn_sched_barrier(0); asm volatile("s_waitcnt lgkmcnt(0)\n\ts_barrier" ::: "memory"); __builtin_amdgcn_sched_barrier(0); } while (0)
#define CFENCE() asm volatile("" ::: "memory")
#define SCHED() __builtin_amdgcn_sched_barrier(0)
__device__ __forceinline__ v4u ld128(__amdgpu_buffer_rsrc_t rs, int off) { return __builtin_amdgcn_raw_buffer_load_b128(rs, off, 0, AUX_SC1); }
__device__ __forceinline__ v2u ld64(__amdgpu_buffer_rsrc_t rs, int off) { return __builtin_amdgcn_raw_buffer_load_b64(rs, off, 0, AUX_SC1); }
__device__ __forceinline__ u32 ld32(__amdgpu_buffer_rsrc_t rs, int off) { return __builtin_amdgcn_raw_buffer_load_b32(rs, off, 0, AUX_SC1); }
__device__ __forceinline__ void st128(v4u v, __amdgpu_buffer_rsrc_t rs, int off) { __builtin_amdgcn_raw_buffer_store_b128(v, rs, off, 0, AUX_SC1); }
__device__ __forceinline__ void st64(v2u v, __amdgpu_buffer_rsrc_t rs, int off) { __builtin_amdgcn_raw_buffer_store_b64(v, rs, off, 0, AUX_SC1); }
__device__ __forceinline__ void st32(u32 v, __amdgpu_buffer_rsrc_t rs, int off) { __builtin_amdgcn_raw_buffer_store_b32(v, rs, off, 0, AUX_SC1); }
__device__ __forceinline__ u32 umax4(v4u v) { u32 a = v.x > v.y ? v.x : v.y, b = v.z > v.w ? v.z : v.w; return a > b ? a : b; }
__device__ __forceinline__ u32 umax2(u32 a, u32 b) { return a > b ? a : b; }

#define STAMP(i) SCHED()
#define STAMP_OUT()

constexpr int L_QP = 0;
constexpr int L_SC = 2048;
constexpr int L_FL = 2560;
constexpr int L_RED = 2816;
constexpr int L_QS = 4096;
constexpr int L_W1 = 8448;
constexpr int L_BYTES = L_W1 + 16384;
constexpr int OOB = 0x7FFFFF00;
#define LDSW() asm volatile("s_waitcnt lgkmcnt(0)" ::: "memory")
#define SPIN_END(cond) if (cond) break; if (++spins > SPIN_CAP) break

__device__ __forceinline__ void check_ready(__amdgpu_buffer_rsrc_t rs, u32 tag, int lane, LAS unsigned char* L) {
  for (u32 spins = 0;;) {
    CFENCE();
    const int o = Q1_OFF + lane * 256 + 4;
    const u32 r0 = ld32(rs, o), r1 = ld32(rs, o + 16384), r2 = ld32(rs, o + 32768), r3 = ld32(rs, o + 49152);
    const bool ok = r0 == tag && r1 == tag && r2 == tag && r3 == tag;
    SPIN_END(__builtin_amdgcn_ballot_w64(!ok) == 0);
    __builtin_amdgcn_s_sleep(8);
  }
  *(volatile LAS u32*)(L + L_FL + 16) = 1u;
}
__device__ __forceinline__ void wait_ready(LAS unsigned char* L) {
  for (u32 spins = 0;;) {
    CFENCE();
    const u32 fl = *(volatile LAS u32*)(L + L_FL + 16);
    SPIN_END(__builtin_amdgcn_readfirstlane(fl) != 0);
    __builtin_amdgcn_s_sleep(2);
  }
}

template <int ROLE>
__device__ __forceinline__ void body(const u16* __restrict__ x, const u16* __restrict__ Kc, const u16* __restrict__ Vc, const u16* __restrict__ wq,
            const u16* __restrict__ wo, const u16* __restrict__ w1, const u16* __restrict__ b1, const u16* __restrict__ w2,
            u16* __restrict__ out, unsigned char* ws, LAS unsigned char* L) {
  const int tid = threadIdx.x, lane = tid & 63, w = __builtin_amdgcn_readfirstlane(tid >> 6);
  const int b = blockIdx.x, g = b & 7, j = b >> 3;
  const __amdgpu_buffer_rsrc_t rs = __builtin_amdgcn_make_buffer_rsrc((void*)ws, 0, WS_BYTES, 0x00020000);
  constexpr bool is_o = ROLE == 0, is_f = ROLE == 1;
  const int h = 2 * g + (j >= 7 ? 1 : 0), nc = j >= 7 ? j - 7 : j;
  const int f = g * 14 + (j - 14), f0 = f * 16;
  const int m = lane & 15, kg = lane >> 4;
  const f32x4 z = {0.f, 0.f, 0.f, 0.f};
  const v4u sent4 = {SENT, SENT, SENT, SENT};
  STAMP(0);
  if (w >= 4) {
    const int a = w - 4;
    const u16* Kp = Kc + ((size_t)g * SK + (SK - WIN) + j * 64 + a * 16 + m) * DH + kg * 8;
    const u16* Vp = Vc + ((size_t)g * SK + (SK - WIN) + j * 64 + (lane >> 3) * 8) * DH + a * 64 + (lane & 7) * 8;
    if (a == 0) *(LAS v4u*)(L + L_QP + lane * 16) = sent4;
    if (a == 1) *(LAS v4u*)(L + L_SC + lane * 16) = lane < 32 ? sent4 : v4u{0u, 0u, 0u, 0u};
    BAR();
    __builtin_amdgcn_s_setprio(1);
    u32 ep = 0;
    if (is_f) ep = ld32(rs, EPOCH_OFF);
    v4u Kr[8], Vr[8];
#pragma unroll
    for (int ks = 0; ks < 8; ++ks) Kr[ks] = ldg<v4u>(Kp + ks * 32);
    SCHED();
#pragma unroll
    for (int i = 0; i < 8; ++i) Vr[i] = ldg<v4u>(Vp + i * DH);
    SCHED();
    if (lane == 0) __hip_atomic_fetch_add((LAS u32*)(L + L_FL + 20), 1u, __ATOMIC_RELAXED, __HIP_MEMORY_SCOPE_WORKGROUP);
    STAMP(1);
    v4u qa[8];
    for (u32 spins = 0;;) {
      CFENCE();
      const v4u fl4 = *(volatile LAS v4u*)(L + L_FL);
      const u32 fl = fl4.x & fl4.y & fl4.z & fl4.w;
      CFENCE();
#pragma unroll
      for (int ks = 0; ks < 8; ++ks) qa[ks] = *(LAS v4u*)(L + L_QS + (m & 7) * 528 + ks * 64 + kg * 16);
      asm volatile("" : "+v"(qa[0]), "+v"(qa[1]), "+v"(qa[2]), "+v"(qa[3]), "+v"(qa[4]), "+v"(qa[5]), "+v"(qa[6]), "+v"(qa[7]));
      SPIN_END(__builtin_amdgcn_readfirstlane(fl) != 0);
      __builtin_amdgcn_s_sleep(1);
    }
    STAMP(2);
    f32x4 acc = z, acc2 = z;
#pragma unroll
    for (int ks = 0; ks < 8; ks += 2) { acc = mfma(qa[ks], Kr[ks], acc); acc2 = mfma(qa[ks + 1], Kr[ks + 1], acc2); }
    {
      acc += acc2;
      float sv = (acc[0] + acc[1]) + (acc[2] + acc[3]);
      sv = bfhi(pkbf(0.f, sv)) * (0.0625f * 1.4426950408889634f);
      if (lane < 32) *(LAS u32*)(L + L_SC + (lane >> 4) * 256 + (a * 16 + m) * 4) = nosent(asu(sv));
    }
    STAMP(3);
    const int hd = (m >> 2) & 1, kbit = m & 1;
    v4u s0, s1;
    for (u32 spins = 0;;) {
      CFENCE();
      s0 = *(LAS v4u*)(L + L_SC + hd * 256 + (kg * 2 + kbit) * 32);
      s1 = *(LAS v4u*)(L + L_SC + hd * 256 + (kg * 2 + kbit) * 32 + 16);
      SPIN_END(__builtin_amdgcn_ballot_w64(umax2(umax4(s0), umax4(s1)) == SENT) == 0);
    }
    float e[8] = {asf(s0[0]), asf(s0[1]), asf(s0[2]), asf(s0[3]), asf(s1[0]), asf(s1[1]), asf(s1[2]), asf(s1[3])};
    float mx = fmaxf(fmaxf(fmaxf(e[0], e[1]), fmaxf(e[2], e[3])), fmaxf(fmaxf(e[4], e[5]), fmaxf(e[6], e[7])));
    mx = fmaxf(mx, dpp<DPP_XOR1>(mx));
    mx = max_x32(max_x16(mx));
#pragma unroll
    for (int i = 0; i < 8; ++i) e[i] = __builtin_amdgcn_exp2f(e[i] - mx);
    float ls = ((e[0] + e[1]) + (e[2] + e[3])) + ((e[4] + e[5]) + (e[6] + e[7]));
    ls += dpp<DPP_XOR1>(ls);
    ls = add_x32(add_x16(ls));
    const float inv = __builtin_amdgcn_rcpf(ls);
    v4u pa;
#pragma unroll
    for (int i = 0; i < 4; ++i) pa[i] = pkbf(e[2 * i] * inv, e[2 * i + 1] * inv);
    const float lse = mx + __builtin_amdgcn_logf(ls);
    st32(nosent(asu(lse)), rs, (a == 0 && (lane == 0 || lane == 4)) ? P2_OFF + b * 2048 + 1024 + hd * 4 : OOB);
    STAMP(4);
    v4u B[8];
#pragma unroll
    for (int ii = 0; ii < 4; ++ii) {
#pragma unroll
      for (int jj = 0; jj < 8; ++jj)
        B[jj][ii] = (jj & 1) ? phi(Vr[2 * ii + 1][jj >> 1], Vr[2 * ii][jj >> 1]) : plo(Vr[2 * ii + 1][jj >> 1], Vr[2 * ii][jj >> 1]);
      SCHED();
    }
    f32x4 c[8];
#pragma unroll
    for (int jj = 0; jj < 8; ++jj) c[jj] = mfma(pa, B[jj], z);
    SCHED();
    float o[8];
#pragma unroll
    for (int jj = 0; jj < 8; ++jj) o[jj] = c[jj][0] + dpp<DPP_ROR8>(c[jj][1]);
    {
      v4u ov;
#pragma unroll
      for (int i = 0; i < 4; ++i) ov[i] = nosent(pkbf(o[2 * i], o[2 * i + 1]));
      st128(ov, rs, (lane < 32 && !(lane & 8)) ? P2_OFF + b * 2048 + (lane >> 4) * 512 + a * 128 + (lane & 7) * 16 : OOB);
    }
    STAMP(5);
    if (is_f && a == 3) {
      const u32 tag = __builtin_amdgcn_readfirstlane(ep) + 1u;
      wait_ready(L);
      v2u hp;
      v4u r0, r1;
      const int o0 = P4_OFF + lane * 2048 + f * 16, o1 = P4_OFF + (lane < 48 ? 64 + lane : lane) * 2048 + f * 16;
      for (u32 spins = 0;;) {
        CFENCE();
        hp = ld64(rs, P3_OFF + g * 16384 + m * 1024 + f * 8);
        r0 = ld128(rs, o0); r1 = ld128(rs, o1);
        asm volatile("" :: "v"(r0), "v"(r1));
        SPIN_END(__builtin_amdgcn_ballot_w64(umax2(hp.x, hp.y) == SENT) == 0);
        __builtin_amdgcn_s_sleep(2);
      }
      BAR();
      for (u32 spins = 0;;) {
        CFENCE();
        r0 = ld128(rs, o0); r1 = ld128(rs, o1);
        SPIN_END(__builtin_amdgcn_ballot_w64(umax2(umax4(r0), umax4(r1)) == SENT) == 0);
      }
      STAMP(8);
      float v[4];
      v[0] = asf(r0.x) + (lane < 48 ? asf(r1.x) : bflo(hp.x));
      v[1] = asf(r0.y) + (lane < 48 ? asf(r1.y) : bfhi(hp.x));
      v[2] = asf(r0.z) + (lane < 48 ? asf(r1.z) : bflo(hp.y));
      v[3] = asf(r0.w) + (lane < 48 ? asf(r1.w) : bfhi(hp.y));
#pragma unroll
      for (int i = 0; i < 4; ++i) v[i] = wave_sum(v[i]);
      if (lane == 0) {
        v2u ov; ov.x = pkbf(v[0], v[1]); ov.y = pkbf(v[2], v[3]);
        *(GAS v2u*)(out + f * 4) = ov;
        if (f == 0) st32(tag, rs, EPOCH_OFF);
      }
      STAMP(9);
    }
    STAMP(10);
    STAMP_OUT();
    __builtin_amdgcn_endpgm();
  } else {
    __builtin_amdgcn_s_setprio(3);
    const int c = tid & 1, rq = tid >> 1, rqc = rq < 112 ? rq : 111;
    const int part = j & 3, cb = j >> 2;
    const int qr = tid >> 3, qrow = part * 112 + (qr < 28 ? qr : 27) * 4;
    if (w == CLRW) {
      st128(sent4, rs, P2_OFF + b * 2048 + lane * 16);
      st128(sent4, rs, lane == 0 ? P2_OFF + b * 2048 + 1024 : OOB);
      if (is_o) st128(sent4, rs, P3_OFF + (lane >> 3) * 16384 + h * 1024 + nc * 128 + (lane & 7) * 16);
      if (is_f) { st128(sent4, rs, P4_OFF + f * 2048 + lane * 16); st128(sent4, rs, P4_OFF + f * 2048 + 1024 + lane * 16); }
    }
    SCHED();
    const v4u xa = ldp<v4u>(x + (lane < 56 ? lane : 55) * 8);
    const v2u xq = ldp<v2u>(x + qrow);
    v4u Wr[4];
    {
      const u16* p = wq + (size_t)qrow * NQ + g * 512 + cb * 64 + (lane & 7) * 8;
#pragma unroll
      for (int i = 0; i < 4; ++i) Wr[i] = ldx<v4u, NT_LOADS>(p + i * NQ);
    }
    const u32 ep = ld32(rs, EPOCH_OFF);
    SCHED();
    BAR();
    float s1 = ((bflo(xa[0]) + bfhi(xa[0])) + (bflo(xa[1]) + bfhi(xa[1]))) + ((bflo(xa[2]) + bfhi(xa[2])) + (bflo(xa[3]) + bfhi(xa[3])));
    s1 = lane < 56 ? s1 : 0.f;
    const float mu = wave_sum(s1) * (1.0f / D);
    float s2 = 0.f, s3 = 0.f;
#pragma unroll
    for (int i = 0; i < 4; ++i) { const float a = bflo(xa[i]) - mu, bb = bfhi(xa[i]) - mu; s2 += a * a; s3 += bb * bb; }
    s2 = lane < 56 ? s2 + s3 : 0.f;
    float rstd = __builtin_amdgcn_rsqf(wave_sum(s2) * (1.0f / D) + 1e-5f);
    rstd = qr < 28 ? rstd : 0.f;
    const u32 xn0 = pkbf((bflo(xq.x) - mu) * rstd, (bfhi(xq.x) - mu) * rstd);
    const u32 xn1 = pkbf((bflo(xq.y) - mu) * rstd, (bfhi(xq.y) - mu) * rstd);
    STAMP(11);
    float a[8];
#pragma unroll
    for (int jj = 0; jj < 4; ++jj) {
      a[2 * jj] = dot2(plo(Wr[3][jj], Wr[2][jj]), xn1, dot2(plo(Wr[1][jj], Wr[0][jj]), xn0, 0.f));
      a[2 * jj + 1] = dot2(phi(Wr[3][jj], Wr[2][jj]), xn1, dot2(phi(Wr[1][jj], Wr[0][jj]), xn0, 0.f));
    }
#pragma unroll
    for (int jj = 0; jj < 8; ++jj) a[jj] += dpp<DPP_ROR8>(a[jj]);
#pragma unroll
    for (int jj = 0; jj < 8; ++jj) a[jj] = add_x16(a[jj]);
#pragma unroll
    for (int jj = 0; jj < 8; ++jj) a[jj] = add_x32(a[jj]);
    if (lane < 8) {
      LAS v4u* p = (LAS v4u*)(L + L_QP + w * 256 + lane * 32);
      p[0] = v4u{nosent(asu(a[0])), nosent(asu(a[1])), nosent(asu(a[2])), nosent(asu(a[3]))};
      p[1] = v4u{nosent(asu(a[4])), nosent(asu(a[5])), nosent(asu(a[6])), nosent(asu(a[7]))};
    }
    const u32 tag = __builtin_amdgcn_readfirstlane(ep) + 1u;
    STAMP(1);
    if (w == 0) {
      v2u p0, p1;
      for (u32 spins = 0;;) {
        CFENCE();
        p0 = *(LAS v2u*)(L + L_QP + (lane >> 5) * 512 + (lane & 31) * 8); p1 = *(LAS v2u*)(L + L_QP + (lane >> 5) * 512 + 256 + (lane & 31) * 8);
        SPIN_END(__builtin_amdgcn_ballot_w64(umax2(umax2(p0.x, p0.y), umax2(p1.x, p1.y)) == SENT) == 0);
      }
      const float q0 = add_x32(asf(p0.x) + asf(p1.x)), q1 = add_x32(asf(p0.y) + asf(p1.y));
      v2u gr; gr.x = pkbf(q0, q1); gr.y = tag;
      st64(gr, rs, lane < 32 ? Q1_OFF + g * 8192 + part * 2048 + cb * 256 + lane * 8 : OOB);
      STAMP(2);
    }
    {
      for (u32 spins = 0;;) {
        CFENCE();
        const u32 fl = *(volatile LAS u32*)(L + L_FL + 20);
        SPIN_END(__builtin_amdgcn_readfirstlane(fl) >= QPOLL_AT);
        __builtin_amdgcn_s_sleep(1);
      }
      if (QDELAY) { for (int i = 0; i < QDELAY; ++i) __builtin_amdgcn_s_sleep(4); }
      v4u g0, g1;
      for (u32 spins = 0;;) {
        CFENCE();
        g0 = ld128(rs, Q1_OFF + g * 8192 + w * 2048 + lane * 32); g1 = ld128(rs, Q1_OFF + g * 8192 + w * 2048 + lane * 32 + 16);
        const bool ok = g0.y == tag && g0.w == tag && g1.y == tag && g1.w == tag;
        SPIN_END(__builtin_amdgcn_ballot_w64(!ok) == 0);
        if (QSLEEP) __builtin_amdgcn_s_sleep(QSLEEP);
      }
      *(LAS v4u*)(L + L_QS + ((lane >> 5) * 4 + w) * 528 + (lane & 31) * 16) = v4u{g0.x, g0.z, g1.x, g1.z};
      CFENCE();
      *(volatile LAS u32*)(L + L_FL + w * 4) = 1u;
      STAMP(3);
    }
    __builtin_amdgcn_s_setprio(0);
    if (is_o) {
      v2u r[16];
      const u16* p = wo + (size_t)(h * 256 + w * 64 + kg * 8) * D + nc * 64 + m * 4;
#pragma unroll
      for (int i = 0; i < 16; ++i) r[i] = ldx<v2u, NT_TAIL>(p + ((i >> 3) * 32 + (i & 7)) * D);
      float xv = bflo(ldp<u16>(x + nc * 64 + lane));
      xv = h == 0 ? xv : 0.f;
      v4u B[2][4];
#pragma unroll
      for (int s = 0; s < 2; ++s)
#pragma unroll
        for (int ii = 0; ii < 4; ++ii) {
          const v2u lo = r[s * 8 + 2 * ii], hi = r[s * 8 + 2 * ii + 1];
          B[s][0][ii] = plo(hi.x, lo.x); B[s][1][ii] = phi(hi.x, lo.x); B[s][2][ii] = plo(hi.y, lo.y); B[s][3][ii] = phi(hi.y, lo.y);
        }
      asm volatile("" : "+v"(xv), "+v"(B[0][0]), "+v"(B[0][1]), "+v"(B[0][2]), "+v"(B[0][3]), "+v"(B[1][0]), "+v"(B[1][1]), "+v"(B[1][2]), "+v"(B[1][3]));
      STAMP(4);
      __builtin_amdgcn_s_setprio(2);
      const int a_off = P2_OFF + (m * 8 + g) * 2048 + (h & 1) * 512 + w * 128 + kg * 16;
      const int s_off = P2_OFF + (kg * 32 + g) * 2048 + 1024 + (h & 1) * 4;
      v4u A[4], S0, S1;
      for (u32 spins = 0;;) {
        CFENCE();
        #pragma unroll
        for (int i = 0; i < 4; ++i) { S0[i] = ld32(rs, s_off + i * 8 * 2048); S1[i] = ld32(rs, s_off + (16 + i) * 8 * 2048); }
        A[0] = ld128(rs, a_off); A[1] = ld128(rs, a_off + 64); A[2] = ld128(rs, a_off + 16 * 8 * 2048); A[3] = ld128(rs, a_off + 16 * 8 * 2048 + 64);
        SPIN_END(__builtin_amdgcn_ballot_w64(umax2(umax4(S0), umax4(S1)) == SENT) == 0);
        asm volatile("" :: "v"(A[0]), "v"(A[1]), "v"(A[2]), "v"(A[3]));
        __builtin_amdgcn_s_sleep(1);
      }
      STAMP(6);
      float e[8];
#pragma unroll
      for (int i = 0; i < 4; ++i) { e[i] = asf(S0[i]); e[4 + i] = asf(S1[i]); }
      float M = fmaxf(fmaxf(fmaxf(e[0], e[1]), fmaxf(e[2], e[3])), fmaxf(fmaxf(e[4], e[5]), fmaxf(e[6], e[7])));
      M = max_x32(max_x16(M));
#pragma unroll
      for (int i = 0; i < 8; ++i) e[i] = __builtin_amdgcn_exp2f(e[i] - M);
      float Ls = ((e[0] + e[1]) + (e[2] + e[3])) + ((e[4] + e[5]) + (e[6] + e[7]));
      Ls = add_x32(add_x16(Ls));
      const float inv = __builtin_amdgcn_rcpf(Ls);
#pragma unroll
      for (int i = 0; i < 8; ++i) { e[i] *= inv; asm volatile("" : "+v"(e[i])); }
      for (u32 spins = 0;;) {
        SPIN_END(__builtin_amdgcn_ballot_w64(umax2(umax2(umax4(A[0]), umax4(A[1])), umax2(umax4(A[2]), umax4(A[3]))) == SENT) == 0);
        CFENCE();
        A[0] = ld128(rs, a_off); A[1] = ld128(rs, a_off + 64); A[2] = ld128(rs, a_off + 16 * 8 * 2048); A[3] = ld128(rs, a_off + 16 * 8 * 2048 + 64);
      }
      STAMP(7);
      f32x4 C0[4], C1[4];
#pragma unroll
      for (int jj = 0; jj < 4; ++jj) { C0[jj] = mfma(A[0], B[0][jj], z); C1[jj] = mfma(A[2], B[0][jj], z); }
#pragma unroll
      for (int jj = 0; jj < 4; ++jj) { C0[jj] = mfma(A[1], B[1][jj], C0[jj]); C1[jj] = mfma(A[3], B[1][jj], C1[jj]); }
      f32x4 v;
#pragma unroll
      for (int jj = 0; jj < 4; ++jj) {
        const float t0 = e[0] * C0[jj][0] + e[1] * C0[jj][1], t1 = e[2] * C0[jj][2] + e[3] * C0[jj][3];
        const float t2 = e[4] * C1[jj][0] + e[5] * C1[jj][1], t3 = e[6] * C1[jj][2] + e[7] * C1[jj][3];
        v[jj] = add_x32(add_x16((t0 + t1) + (t2 + t3)));
      }
      if (lane < 16) *(LAS f32x4*)(L + L_RED + w * 256 + lane * 16) = v;
      BAR();
      const LAS float* rp = (const LAS float*)(L + L_RED + lane * 4);
      const float s = ((rp[0] + rp[64]) + (rp[128] + rp[192])) + xv;
      const u32 pk = nosent(pkbf(s, dpp<DPP_XOR1>(s)));
      const u32 pkx = asu(dpp<DPP_XOR1>(asf(pk)));
      const u32 pk2 = (lane & 1) ? pkx : pk;
      st32(pk2, rs, P3_OFF + (2 * w + (lane & 1)) * 16384 + h * 1024 + (nc * 64 + (lane & ~1)) * 2);
      STAMP(8);
    } else if (is_f) {
      v4u W1r[4];
      u32 W2r[16];
      {
        const u16* p = w1 + (size_t)(rqc * 4) * F + f0 + c * 8;
#pragma unroll
        for (int i = 0; i < 4; ++i) W1r[i] = ldx<v4u, NT_WQ>(p + i * F);
        const u16* p2 = w2 + (size_t)f0 * D + 2 * (tid < 224 ? tid : 223);
#pragma unroll
        for (int k = 0; k < 16; ++k) W2r[k] = ldx<u32, NT_TAIL>(p2 + k * D);
      }
      float b1v = bflo(ldp<u16>(b1 + f0 + m));
#pragma unroll
      for (int i = 0; i < 4; ++i) *(LAS v4u*)(L + L_W1 + (rq * 4 + i) * 32 + c * 16) = rq < 112 ? W1r[i] : v4u{0u, 0u, 0u, 0u};
      LDSW();
      v4u Bf[4];
#pragma unroll
      for (int s = 0; s < 4; ++s)
#pragma unroll
        for (int ii = 0; ii < 4; ++ii) {
          const int row = (4 * w + s) * 32 + kg * 8 + 2 * ii;
          const u32 lo = *(LAS u16*)(L + L_W1 + row * 32 + m * 2), hi = *(LAS u16*)(L + L_W1 + row * 32 + 32 + m * 2);
          Bf[s][ii] = lo | (hi << 16);
        }
      const v4u ones = {0x3F803F80u, 0x3F803F80u, 0x3F803F80u, 0x3F803F80u};
      f32x4 cs = z;
#pragma unroll
      for (int s = 0; s < 4; ++s) cs = mfma(ones, Bf[s], cs);
      float csv = cs[0];
      u32 Wp[2][8];
#pragma unroll
      for (int k = 0; k < 8; ++k) {
        Wp[0][k] = plo(W2r[2 * k + 1], W2r[2 * k]); Wp[1][k] = phi(W2r[2 * k + 1], W2r[2 * k]);
        asm volatile("" : "+v"(Wp[0][k]), "+v"(Wp[1][k]));
      }
      asm volatile("" : "+v"(csv), "+v"(b1v));
      STAMP(4);
      __builtin_amdgcn_s_setprio(2);
      if (w == 3) check_ready(rs, tag, lane, L); else wait_ready(L);
      STAMP(5);
      const int a_off = P3_OFF + g * 16384 + m * 1024 + w * 256 + kg * 16;
      const int a_off2 = w < 3 ? a_off + 128 : OOB;
      v4u A[4];
      for (u32 spins = 0;;) {
        CFENCE();
        A[0] = ld128(rs, a_off); A[1] = ld128(rs, a_off + 64); A[2] = ld128(rs, a_off2); A[3] = ld128(rs, a_off2 + 64);
        SPIN_END(__builtin_amdgcn_ballot_w64(umax2(umax2(umax4(A[0]), umax4(A[1])), umax2(umax4(A[2]), umax4(A[3]))) == SENT) == 0);
      }
      STAMP(7);
      f32x4 U = z, G = z, S = z;
#pragma unroll
      for (int s = 0; s < 4; ++s) { G = mfma(A[s], A[s], G); S = mfma(A[s], ones, S); U = mfma(A[s], Bf[s], U); }
      f32x4 r;
      r[0] = add_x32(add_x16((U[0] + U[1]) + (U[2] + U[3])));
      r[1] = csv;
      r[2] = add_x32(add_x16((S[0] + S[1]) + (S[2] + S[3])));
      r[3] = wave_sum((G[0] + G[1]) + (G[2] + G[3]));
      if (lane < 16) *(LAS f32x4*)(L + L_RED + w * 256 + lane * 16) = r;
      BAR();
      const LAS f32x4* rp = (const LAS f32x4*)(L + L_RED + m * 16);
      const f32x4 t = (rp[0] + rp[16]) + (rp[32] + rp[48]);
      const float mu2 = t[2] * (1.0f / D);
      const float var = fmaxf(t[3] * (1.0f / D) - mu2 * mu2, 0.f);
      const float rstd2 = __builtin_amdgcn_rsqf(var + 1e-5f);
      const float u = (t[0] - mu2 * t[1]) * rstd2 + b1v;
      const float n = __builtin_amdgcn_exp2f(-fabsf(u) * 1.4426950408889634f);
      const float num = u > 0.f ? 1.f + 2.f * n : n * n + 2.f * n;
      const float den = u > 0.f ? num + 2.f * n * n : num + 2.f;
      const float act = u * num * __builtin_amdgcn_rcpf(den);
      const u32 ap = pkbf(act, dpp<DPP_XOR1>(act));
      float o0 = 0.f, o1 = 0.f, o2 = 0.f, o3 = 0.f;
#pragma unroll
      for (int k = 0; k < 8; k += 2) {
        const u32 a0 = __builtin_amdgcn_readlane(ap, 2 * k), a1 = __builtin_amdgcn_readlane(ap, 2 * k + 2);
        o0 = dot2(Wp[0][k], a0, o0); o1 = dot2(Wp[1][k], a0, o1);
        o2 = dot2(Wp[0][k + 1], a1, o2); o3 = dot2(Wp[1][k + 1], a1, o3);
      }
      v2u ov; ov.x = nosent(asu(o0 + o2)); ov.y = nosent(asu(o1 + o3));
      st64(ov, rs, tid < 224 ? P4_OFF + f * 2048 + tid * 8 : OOB);
      STAMP(8);
    }
  }
  STAMP(10);
  STAMP_OUT();
  __builtin_amdgcn_endpgm();
}

__global__ void __launch_bounds__(NTHR, 2)
fused_block(const u16* __restrict__ x, const u16* __restrict__ Kc, const u16* __restrict__ Vc, const u16* __restrict__ wq,
            const u16* __restrict__ wo, const u16* __restrict__ w1, const u16* __restrict__ b1, const u16* __restrict__ w2,
            u16* __restrict__ out, unsigned char* ws) {
  __shared__ __attribute__((aligned(16))) unsigned char smem[L_BYTES];
  LAS unsigned char* L = (LAS unsigned char*)smem;
  const int j = blockIdx.x >> 3;
  if (j < 14) body<0>(x, Kc, Vc, wq, wo, w1, b1, w2, out, ws, L);
  else if (j < 28) body<1>(x, Kc, Vc, wq, wo, w1, b1, w2, out, ws, L);
  else body<2>(x, Kc, Vc, wq, wo, w1, b1, w2, out, ws, L);
}

extern "C" void kernel_launch(void* const* d_in, const int* in_sizes, int n_in, void* d_out, int out_size, void* d_ws,
                              size_t ws_size, hipStream_t stream) {
  fused_block<<<NWG, NTHR, 0, stream>>>((const u16*)d_in[0], (const u16*)d_in[1], (const u16*)d_in[2], (const u16*)d_in[3],
                                        (const u16*)d_in[4], (const u16*)d_in[6], (const u16*)d_in[7], (const u16*)d_in[8],
                                        (u16*)d_out, (unsigned char*)d_ws);
}
```

```cpp
#include <hip/hip_runtime.h>
#include <stdint.h>

#define GAS __attribute__((address_space(1)))
#define LAS __attribute__((address_space(3)))
typedef unsigned u32;
typedef unsigned short u16;
typedef u32 v4u __attribute__((ext_vector_type(4)));
typedef u32 v2u __attribute__((ext_vector_type(2)));
typedef float f32x4 __attribute__((ext_vector_type(4)));
typedef float f32x2 __attribute__((ext_vector_type(2)));
typedef short bf16x8 __attribute__((ext_vector_type(8)));
typedef short s16x4 __attribute__((ext_vector_type(4)));
typedef __bf16 bf16x2 __attribute__((ext_vector_type(2)));

#define NT_LOADS 1
#define NT_WQ 0
#define NT_TAIL 1
#define NK0 8
#ifndef NV0
#define NV0 2
#endif
#define TAIL_GATE 2
#define PRETOUCH 0
#define QDELAY 2
#define QSLEEP 0
#define WO_EARLY 0
#define ODELAY 0
#define KDELAY 2
#define CODEWARM 1
#define CODE_BYTES 10496
#define PDELAY 18
#define NKW 2
#define VPRE 0
#define FSTAMP 0
#define W2_EARLY 1
#define ROT 1
#define MAPB 1
#define GSH 1
#define RDELAY 4
#define FDELAY 0
#define SELP 1
#define STAMPS 0

constexpr int D = 448, DH = 256, SK = 8192, WIN = 2048, F = 1792, NQ = 4096;
constexpr int NWG = 256, NTHR = 576;
constexpr int AUX_SC1 = 16;
constexpr u32 SENT = 0xFFFFFFFFu;
constexpr u32 SPIN_CAP = 1u << 18;
constexpr int EPOCH_OFF = 0;
constexpr int RDY_OFF = 1024;
constexpr int Q1_OFF = 4096;
constexpr int S2_OFF = Q1_OFF + 65536;
constexpr int P2S = 1152, P3S = 896, P3R = 16 * P3S + 128, P4S = 1920;
constexpr int P2_OFF = 81920;
constexpr int P3_OFF = P2_OFF + 256 * P2S;
constexpr int P4_OFF = P3_OFF + 8 * P3R;
constexpr int WS_BYTES = P4_OFF + 112 * P4S;
constexpr int STAMP_OFF = 2 << 20;

__device__ __forceinline__ float asf(u32 u) { return __builtin_bit_cast(float, u); }
__device__ __forceinline__ u32 asu(float f) { return __builtin_bit_cast(u32, f); }
__device__ __forceinline__ float bflo(u32 w) { return asf(w << 16); }
__device__ __forceinline__ float bfhi(u32 w) { return asf(w & 0xFFFF0000u); }
__device__ __forceinline__ u32 pkbf(float lo, float hi) {
  bf16x2 r; r.x = (__bf16)lo; r.y = (__bf16)hi; return __builtin_bit_cast(u32, r);
}
__device__ __forceinline__ u32 nosent(u32 v) { return v < 0xFFFFFFFEu ? v : 0xFFFFFFFEu; }
template <int CTRL> __device__ __forceinline__ float dpp(float x) {
  return asf(__builtin_amdgcn_update_dpp(0, asu(x), CTRL, 0xF, 0xF, true));
}
#define DPP_XOR1 0xB1
#define DPP_XOR2 0x4E
#define DPP_ROR4 0x124
#define DPP_ROR8 0x128
__device__ __forceinline__ float add_x16(float x) {
  auto r = __builtin_amdgcn_permlane16_swap(asu(x), asu(x), false, false);
  return asf(r[0]) + asf(r[1]);
}
__device__ __forceinline__ float add_x32(float x) {
  auto r = __builtin_amdgcn_permlane32_swap(asu(x), asu(x), false, false);
  return asf(r[0]) + asf(r[1]);
}
__device__ __forceinline__ float max_x16(float x) {
  auto r = __builtin_amdgcn_permlane16_swap(asu(x), asu(x), false, false);
  return fmaxf(asf(r[0]), asf(r[1]));
}
__device__ __forceinline__ float max_x32(float x) {
  auto r = __builtin_amdgcn_permlane32_swap(asu(x), asu(x), false, false);
  return fmaxf(asf(r[0]), asf(r[1]));
}
__device__ __forceinline__ float row_sum16(float x) {
  x += dpp<DPP_XOR1>(x); x += dpp<DPP_XOR2>(x); x += dpp<DPP_ROR4>(x); x += dpp<DPP_ROR8>(x); return x;
}
__device__ __forceinline__ float wave_sum(float x) { return add_x32(add_x16(row_sum16(x))); }
__device__ __forceinline__ f32x4 mfma(v4u a, v4u b, f32x4 c) {
  return __builtin_amdgcn_mfma_f32_16x16x32_bf16(__builtin_bit_cast(bf16x8, a), __builtin_bit_cast(bf16x8, b), c, 0, 0, 0);
}
__device__ __forceinline__ float dot2(u32 a, u32 b, float c) {
  return __builtin_amdgcn_fdot2_f32_bf16(__builtin_bit_cast(bf16x2, a), __builtin_bit_cast(bf16x2, b), c, false);
}
__device__ __forceinline__ u32 plo(u32 hi, u32 lo) { return __builtin_amdgcn_perm(hi, lo, 0x05040100u); }
__device__ __forceinline__ u32 phi(u32 hi, u32 lo) { return __builtin_amdgcn_perm(hi, lo, 0x07060302u); }
template <typename T> __device__ __forceinline__ T ldg(const void* p) {
  return __builtin_nontemporal_load((const GAS T*)p);
}
template <typename T, int NT> __device__ __forceinline__ T ldx(const void* p) {
  if (NT) return __builtin_nontemporal_load((const GAS T*)p); else return *(const GAS T*)p;
}
template <typename T> __device__ __forceinline__ T ldp(const void* p) { return *(const GAS T*)p; }
#define BAR() do { __builtin_amdgcn_sched_barrier(0); asm volatile("s_waitcnt lgkmcnt(0)\n\ts_barrier" ::: "memory"); __builtin_amdgcn_sched_barrier(0); } while (0)
#define CFENCE() asm volatile("" ::: "memory")
#define SCHED() __builtin_amdgcn_sched_barrier(0)
__device__ __forceinline__ v4u ld128(__amdgpu_buffer_rsrc_t rs, int off) { return __builtin_amdgcn_raw_buffer_load_b128(rs, off, 0, AUX_SC1); }
__device__ __forceinline__ v2u ld64(__amdgpu_buffer_rsrc_t rs, int off) { return __builtin_amdgcn_raw_buffer_load_b64(rs, off, 0, AUX_SC1); }
__device__ __forceinline__ u32 ld32(__amdgpu_buffer_rsrc_t rs, int off) { return __builtin_amdgcn_raw_buffer_load_b32(rs, off, 0, AUX_SC1); }
__device__ __forceinline__ void st128(v4u v, __amdgpu_buffer_rsrc_t rs, int off) { __builtin_amdgcn_raw_buffer_store_b128(v, rs, off, 0, AUX_SC1); }
__device__ __forceinline__ void st64(v2u v, __amdgpu_buffer_rsrc_t rs, int off) { __builtin_amdgcn_raw_buffer_store_b64(v, rs, off, 0, AUX_SC1); }
__device__ __forceinline__ void st32(u32 v, __amdgpu_buffer_rsrc_t rs, int off) { __builtin_amdgcn_raw_buffer_store_b32(v, rs, off, 0, AUX_SC1); }
__device__ __forceinline__ u32 umax4(v4u v) { u32 a = v.x > v.y ? v.x : v.y, b = v.z > v.w ? v.z : v.w; return a > b ? a : b; }
__device__ __forceinline__ u32 umax2(u32 a, u32 b) { return a > b ? a : b; }

#define STAMP(i) SCHED()
#define STAMP_OUT()

constexpr int L_QP = 0;
constexpr int L_WT = 1024;
constexpr int L_SC = 2048;
constexpr int L_FL = 2560;
constexpr int L_RED = 2816;
constexpr int L_QS = 4096;
constexpr int L_W1 = 8448;
constexpr int L_BYTES = L_W1 + 16384;
constexpr int OOB = 0x7FFFFF00;
#define LDSW() asm volatile("s_waitcnt lgkmcnt(0)" ::: "memory")
#define SPIN_END(cond) if (cond) break; if (++spins > SPIN_CAP) break

__device__ __forceinline__ void wait_flag(LAS unsigned char* L, int idx, u32 n) {
  for (u32 spins = 0;;) {
    CFENCE();
    const u32 fl = *(volatile LAS u32*)(L + L_FL + idx * 4);
    SPIN_END(__builtin_amdgcn_readfirstlane(fl) >= n);
    __builtin_amdgcn_s_sleep(1);
  }
}
__device__ __forceinline__ void bump_flag(LAS unsigned char* L, int idx, int lane) {
  if (lane == 0) __hip_atomic_fetch_add((LAS u32*)(L + L_FL + idx * 4), 1u, __ATOMIC_RELAXED, __HIP_MEMORY_SCOPE_WORKGROUP);
}

typedef u32 v16u __attribute__((ext_vector_type(16)));
__device__ __forceinline__ const u16* mkptr(u32 lo, u32 hi) { return (const u16*)(((unsigned long long)hi << 32) | lo); }
#define ARGS() \
  v16u ka; v4u kb; \
  asm volatile("s_load_dwordx16 %0, %2, 0x0\n\ts_load_dwordx4 %1, %2, 0x40\n\ts_waitcnt lgkmcnt(0)" : "=&s"(ka), "=&s"(kb) : "s"(__builtin_amdgcn_kernarg_segment_ptr())); \
  const u16 *x = mkptr(ka[0], ka[1]), *Kc = mkptr(ka[2], ka[3]), *Vc = mkptr(ka[4], ka[5]), *wq = mkptr(ka[6], ka[7]); \
  const u16 *wo = mkptr(ka[8], ka[9]), *w1 = mkptr(ka[10], ka[11]), *b1 = mkptr(ka[12], ka[13]), *w2 = mkptr(ka[14], ka[15]); \
  u16* out = (u16*)mkptr(kb[0], kb[1]); \
  unsigned char* ws = (unsigned char*)mkptr(kb[2], kb[3]); \
  const __amdgpu_buffer_rsrc_t rs = __builtin_amdgcn_make_buffer_rsrc((void*)ws, 0, WS_BYTES, 0x00020000); \
  (void)x; (void)Kc; (void)Vc; (void)wq; (void)wo; (void)w1; (void)b1; (void)w2; (void)out
#define FIN() STAMP(10); STAMP_OUT(); __builtin_amdgcn_endpgm()

__global__ void __launch_bounds__(NTHR, 3)
fused_block(const u16*, const u16*, const u16*, const u16*, const u16*, const u16*, const u16*, const u16*, u16*, unsigned char*) {
  unsigned long long pc0;
  asm volatile("s_getpc_b64 %0" : "=s"(pc0));
  __shared__ __attribute__((aligned(16))) unsigned char smem[L_BYTES];
  LAS unsigned char* L = (LAS unsigned char*)smem;
  const int tid = threadIdx.x, lane = tid & 63, w = __builtin_amdgcn_readfirstlane(tid >> 6);
  const int b = blockIdx.x, g = b & 7, j = b >> 3;
  const bool is_o = j < 28 && g < 4, is_f = j < 28 && g >= 4;
  const int jo = j >= 14 ? j - 14 : j, gs = j >= 14 ? g + 4 : (g + 1) & 3;
  const int f = (g - 4) * 28 + j, rp3 = (g - 4) * 2 + (j & 1);
  const int h = 2 * gs + (jo >= 7 ? 1 : 0), nc = jo >= 7 ? jo - 7 : jo;
  const int f0 = f * 16;
  const int m = lane & 15, kg = lane >> 4;
  const f32x4 z = {0.f, 0.f, 0.f, 0.f};
  const v4u sent4 = {SENT, SENT, SENT, SENT};
  STAMP(0);
  if (__builtin_expect(w >= 8, 0)) {
    ARGS();
    const u32 ep = ld32(rs, EPOCH_OFF);
    st128(sent4, rs, P2_OFF + (g * 32 + j) * P2S + lane * 16);
    st128(sent4, rs, lane < 8 ? P2_OFF + (g * 32 + j) * P2S + 1024 + lane * 16 : OOB);
    st128(sent4, rs, is_o ? P3_OFF + (lane >> 3) * P3R + h * P3S + nc * 128 + (lane & 7) * 16 : is_f ? P4_OFF + f * P4S + lane * 16 : OOB);
    st128(sent4, rs, (is_f && lane < 56) ? P4_OFF + f * P4S + 1024 + lane * 16 : OOB);
    SCHED();
    BAR();
    asm volatile("s_waitcnt vmcnt(0)" ::: "memory");
    const u32 tag = __builtin_amdgcn_readfirstlane(ep) + 1u;
    st32(tag, rs, lane == 0 ? RDY_OFF + (g * 32 + j) * 4 : OOB);
    SCHED();
    STAMP(1);
    if (j >= 28) {
      const unsigned char* cp = (const unsigned char*)(pc0 & ~255ull) + (j - 28) * (CODE_BYTES / 4) + (lane < CODE_BYTES / 256 ? lane : 0) * 64;
      const u32 cv = ldp<u32>(cp);
      asm volatile("" :: "v"(cv));
    }
    if (is_o || is_f) {
      wait_flag(L, is_o ? 6 : 3, 1);
      for (int i = 0; i < RDELAY; ++i) __builtin_amdgcn_s_sleep(4);
      const int rdy_off = RDY_OFF + (is_o ? gs * 128 + (lane & 7) * 16 : lane * 16);
      for (u32 spins = 0;;) {
        CFENCE();
        const v4u r = ld128(rs, rdy_off);
        SPIN_END(__builtin_amdgcn_ballot_w64(!(r.x == tag && r.y == tag && r.z == tag && r.w == tag)) == 0);
        __builtin_amdgcn_s_sleep(8);
      }
      CFENCE();
      *(volatile LAS u32*)(L + L_FL + 16) = 1u;
      STAMP(5);
    }
    if (is_o) {
      __builtin_amdgcn_s_setprio(2);
      wait_flag(L, 7, 4);
      for (int i = 0; i < ODELAY; ++i) __builtin_amdgcn_s_sleep(1);
      const int s_off = P2_OFF + (gs * 32 + (lane & 31)) * P2S + 1024 + (h & 1) * 64;
      u32 sv = SENT;
      for (u32 spins = 0;;) {
        CFENCE();
        if (sv == SENT && lane < 32) sv = ld32(rs, s_off);
        SPIN_END((u32)__builtin_amdgcn_ballot_w64(sv == SENT) == 0);
      }
      STAMP(6);
      float M = asf(sv);
      M = fmaxf(M, dpp<DPP_XOR1>(M)); M = fmaxf(M, dpp<DPP_XOR2>(M)); M = fmaxf(M, dpp<DPP_ROR4>(M)); M = fmaxf(M, dpp<DPP_ROR8>(M));
      M = max_x16(M);
      const float e = __builtin_amdgcn_exp2f(asf(sv) - M);
      const float Ls = add_x16(row_sum16(e));
      if (lane < 32) *(volatile LAS u32*)(L + L_WT + lane * 4) = nosent(asu(e * __builtin_amdgcn_rcpf(Ls)));
      STAMP(7);
    }
    if (is_f) {
      __builtin_amdgcn_s_setprio(2);
      const int fo = (f >> 5) * 512 + (f & 1) * 256 + ((f >> 1) & 15) * 16;
      const int o0 = P4_OFF + lane * P4S + fo, o1 = lane < 48 ? P4_OFF + (64 + lane) * P4S + fo : OOB;
      BAR();
      const v2u hp = ld64(rs, P3_OFF + rp3 * P3R + m * P3S + f * 8);
      if (PDELAY) __builtin_amdgcn_s_sleep(PDELAY);
      v4u r0 = sent4, r1 = sent4;
      bool ba = true, bb = true;
      for (u32 spins = 0;;) {
        CFENCE();
        if (!SELP || ba) r0 = ld128(rs, o0);
        if (!SELP || bb) r1 = ld128(rs, o1);
        ba = umax4(r0) == SENT; bb = umax4(r1) == SENT;
        SPIN_END(__builtin_amdgcn_ballot_w64(ba || bb) == 0);
      }
      STAMP(8);
      float v[4];
      v[0] = asf(r0.x) + (lane < 48 ? asf(r1.x) : bflo(hp.x));
      v[1] = asf(r0.y) + (lane < 48 ? asf(r1.y) : bfhi(hp.x));
      v[2] = asf(r0.z) + (lane < 48 ? asf(r1.z) : bflo(hp.y));
      v[3] = asf(r0.w) + (lane < 48 ? asf(r1.w) : bfhi(hp.y));
      auto t0 = __builtin_amdgcn_permlane32_swap(asu(v[0]), asu(v[2]), false, false);
      auto t1 = __builtin_amdgcn_permlane32_swap(asu(v[1]), asu(v[3]), false, false);
      const float u0 = row_sum16(add_x16(asf(t0[0]) + asf(t0[1]))), u1 = row_sum16(add_x16(asf(t1[0]) + asf(t1[1])));
      if ((lane & 31) == 0) {
        *(GAS u32*)(out + f * 4 + (lane >> 4)) = pkbf(u0, u1);
        if (f == 0 && lane == 0) st32(tag, rs, EPOCH_OFF);
      }
      STAMP(9);
    }
    FIN();
  }
  if (__builtin_expect(w >= 4, 0)) {
    const int a = w - 4;
    ARGS();
    const u16* Kp = Kc + ((size_t)g * SK + (SK - WIN) + j * 64 + a * 16 + m) * DH + kg * 8;
    const u16* Vp = Vc + ((size_t)g * SK + (SK - WIN) + j * 64 + (lane >> 3) * 8) * DH + a * 64 + (lane & 7) * 8;
    if (a < 3) *(LAS v4u*)(L + (tid - 256) * 16) = tid < 416 ? sent4 : v4u{0u, 0u, 0u, 0u};
    BAR();
    __builtin_amdgcn_s_setprio(1);
    v4u Kr[8], Vr[8];
#pragma unroll
    for (int i = 0; i < VPRE; ++i) Vr[i] = ldg<v4u>(Vp + i * DH);
    SCHED();
    if (a >= NKW) wait_flag(L, 5, 1);
    SCHED();
#pragma unroll
    for (int ks = 0; ks < 8; ++ks) Kr[ks] = ldg<v4u>(Kp + ks * 32);
    SCHED();
    if (a < NKW) wait_flag(L, 5, 1);
    SCHED();
#pragma unroll
    for (int i = VPRE; i < NV0; ++i) Vr[i] = ldg<v4u>(Vp + i * DH);
    SCHED();
    if (NV0 < 8) {
      wait_flag(L, 6, 4);
      SCHED();
#pragma unroll
      for (int i = NV0; i < 8; ++i) Vr[i] = ldg<v4u>(Vp + i * DH);
      SCHED();
    }
    bump_flag(L, 8, lane);
    STAMP(1);
    for (u32 spins = 0;;) {
      CFENCE();
      const v4u fl4 = *(volatile LAS v4u*)(L + L_FL);
      SPIN_END(__builtin_amdgcn_readfirstlane(fl4.x & fl4.y & fl4.z & fl4.w) != 0);
    }
    CFENCE();
    v4u qa[8];
#pragma unroll
    for (int ks = 0; ks < 8; ++ks) qa[ks] = *(LAS v4u*)(L + L_QS + (m & 7) * 528 + ks * 64 + kg * 16);
    STAMP(2);
    f32x4 acc = z, acc2 = z;
#pragma unroll
    for (int ks = 0; ks < 8; ks += 2) { acc = mfma(qa[ks], Kr[ks], acc); acc2 = mfma(qa[ks + 1], Kr[ks + 1], acc2); }
    {
      acc += acc2;
      float sv = (acc[0] + acc[1]) + (acc[2] + acc[3]);
      sv = bfhi(pkbf(0.f, sv)) * (0.0625f * 1.4426950408889634f);
      float mw = fmaxf(sv, dpp<DPP_XOR1>(sv));
      mw = fmaxf(mw, dpp<DPP_XOR2>(mw)); mw = fmaxf(mw, dpp<DPP_ROR4>(mw)); mw = fmaxf(mw, dpp<DPP_ROR8>(mw));
      const float p = __builtin_amdgcn_exp2f(sv - mw);
      if (lane < 32) *(volatile LAS u16*)(L + L_SC + (lane >> 4) * 128 + (a * 16 + m) * 2) = (u16)(pkbf(p, p) & 0x7FFFu);
      const float lw = row_sum16(p);
      if (lane < 32 && m < 2) *(volatile LAS u32*)(L + L_SC + 256 + (lane >> 4) * 32 + m * 16 + a * 4) = nosent(asu(m ? lw : mw));
    }
    STAMP(3);
    v4u B[8];
#pragma unroll
    for (int ii = 0; ii < 4; ++ii) {
#pragma unroll
      for (int jj = 0; jj < 8; ++jj)
        B[jj][ii] = (jj & 1) ? phi(Vr[2 * ii + 1][jj >> 1], Vr[2 * ii][jj >> 1]) : plo(Vr[2 * ii + 1][jj >> 1], Vr[2 * ii][jj >> 1]);
      SCHED();
    }
    STAMP(7);
    v4u pa;
    for (u32 spins = 0;;) {
      CFENCE();
      pa = *(LAS v4u*)(L + L_SC + (m >> 3) * 128 + (kg * 2 + ((m >> 2) & 1)) * 16);
      SPIN_END(__builtin_amdgcn_ballot_w64((((pa.x | pa.y) | (pa.z | pa.w)) & 0x80008000u) != 0) == 0);
    }
    v4u sm = *(LAS v4u*)(L + L_SC + 256 + (kg >> 1) * 32), sl = *(LAS v4u*)(L + L_SC + 256 + (kg >> 1) * 32 + 16);
    const u32 keep = (m & 3) == kg ? 0xFFFFFFFFu : 0u;
    pa &= keep;
    f32x4 c[8];
#pragma unroll
    for (int jj = 0; jj < 8; ++jj) c[jj] = mfma(pa, B[jj], z);
    for (u32 spins = 0;;) {
      SPIN_END(__builtin_amdgcn_ballot_w64(umax2(umax4(sm), umax4(sl)) == SENT) == 0);
      CFENCE();
      sm = *(LAS v4u*)(L + L_SC + 256 + (kg >> 1) * 32); sl = *(LAS v4u*)(L + L_SC + 256 + (kg >> 1) * 32 + 16);
    }
    STAMP(4);
    const float M = fmaxf(fmaxf(asf(sm.x), asf(sm.y)), fmaxf(asf(sm.z), asf(sm.w)));
    float fw[4];
#pragma unroll
    for (int i = 0; i < 4; ++i) fw[i] = __builtin_amdgcn_exp2f(asf(sm[i]) - M);
    const float Ls = (fw[0] * asf(sl.x) + fw[1] * asf(sl.y)) + (fw[2] * asf(sl.z) + fw[3] * asf(sl.w));
    const float inv = __builtin_amdgcn_rcpf(Ls);
    float o[8];
#pragma unroll
    for (int jj = 0; jj < 8; ++jj) o[jj] = (fw[0] * c[jj][0] + fw[1] * c[jj][1]) + (fw[2] * c[jj][2] + fw[3] * c[jj][3]);
    float os[4];
#pragma unroll
    for (int jj = 0; jj < 4; ++jj) {
      auto r = __builtin_amdgcn_permlane16_swap(asu(o[jj]), asu(o[jj + 4]), false, false);
      os[jj] = (asf(r[0]) + dpp<DPP_ROR8>(asf(r[1]))) * inv;
    }
    {
      v2u ov; ov.x = nosent(pkbf(os[0], os[1])); ov.y = nosent(pkbf(os[2], os[3]));
      st64(ov, rs, !(lane & 8) ? P2_OFF + (g * 32 + j) * P2S + (kg >> 1) * 512 + a * 128 + (kg & 1) * 64 + (lane & 7) * 8 : OOB);
      const float lse = M + __builtin_amdgcn_logf(Ls);
      st32(nosent(asu(lse)), rs, (a == 0 && !(kg & 1)) ? P2_OFF + (g * 32 + j) * P2S + 1024 + (kg >> 1) * 64 + m * 4 : OOB);
    }
    SCHED();
    bump_flag(L, 7, lane);
    STAMP(5);
    FIN();
  }
  {
    __builtin_amdgcn_s_setprio(3);
    const int part = j & 3, cb = j >> 2;
    const int qr = tid >> 3, qrow = part * 112 + (qr < 28 ? qr : 27) * 4;
    ARGS();
    const v4u xa = ldp<v4u>(x + (lane < 56 ? lane : 55) * 8);
    const v2u xq = ldp<v2u>(x + qrow);
    SCHED();
    v4u Wr[4];
    {
      const u16* p = wq + (size_t)qrow * NQ + g * 512 + cb * 64 + (lane & 7) * 8;
#pragma unroll
      for (int i = 0; i < 4; ++i) Wr[i] = ldx<v4u, NT_LOADS>(p + i * NQ);
    }
    const u32 ep = ld32(rs, EPOCH_OFF);
    SCHED();
    BAR();
    float s1 = ((bflo(xa[0]) + bfhi(xa[0])) + (bflo(xa[1]) + bfhi(xa[1]))) + ((bflo(xa[2]) + bfhi(xa[2])) + (bflo(xa[3]) + bfhi(xa[3])));
    s1 = lane < 56 ? s1 : 0.f;
    const float mu = wave_sum(s1) * (1.0f / D);
    float s2 = 0.f, s3 = 0.f;
#pragma unroll
    for (int i = 0; i < 4; ++i) { const float a = bflo(xa[i]) - mu, bb = bfhi(xa[i]) - mu; s2 += a * a; s3 += bb * bb; }
    s2 = lane < 56 ? s2 + s3 : 0.f;
    float rstd = __builtin_amdgcn_rsqf(wave_sum(s2) * (1.0f / D) + 1e-5f);
    rstd = qr < 28 ? rstd : 0.f;
    const u32 xn0 = pkbf((bflo(xq.x) - mu) * rstd, (bfhi(xq.x) - mu) * rstd);
    const u32 xn1 = pkbf((bflo(xq.y) - mu) * rstd, (bfhi(xq.y) - mu) * rstd);
    STAMP(11);
    float a[8];
#pragma unroll
    for (int jj = 0; jj < 4; ++jj) {
      a[2 * jj] = dot2(plo(Wr[3][jj], Wr[2][jj]), xn1, dot2(plo(Wr[1][jj], Wr[0][jj]), xn0, 0.f));
      a[2 * jj + 1] = dot2(phi(Wr[3][jj], Wr[2][jj]), xn1, dot2(phi(Wr[1][jj], Wr[0][jj]), xn0, 0.f));
    }
    float q2[2];
    {
      float t[4];
#pragma unroll
      for (int jj = 0; jj < 4; ++jj) {
        auto r = __builtin_amdgcn_permlane32_swap(asu(a[jj]), asu(a[jj + 4]), false, false);
        t[jj] = asf(r[0]) + asf(r[1]);
      }
#pragma unroll
      for (int jj = 0; jj < 2; ++jj) {
        auto r = __builtin_amdgcn_permlane16_swap(asu(t[jj]), asu(t[jj + 2]), false, false);
        q2[jj] = asf(r[0]) + asf(r[1]);
        q2[jj] += dpp<DPP_ROR8>(q2[jj]);
      }
    }
    if (!(lane & 8)) *(LAS v2u*)(L + L_QP + w * 256 + (lane & 7) * 32 + kg * 8) = v2u{nosent(asu(q2[0])), nosent(asu(q2[1]))};
    const u32 tag = __builtin_amdgcn_readfirstlane(ep) + 1u;
    STAMP(1);
    if (w == 0) {
      v2u p0, p1;
      for (u32 spins = 0;;) {
        CFENCE();
        p0 = *(LAS v2u*)(L + L_QP + (lane >> 5) * 512 + (lane & 31) * 8); p1 = *(LAS v2u*)(L + L_QP + (lane >> 5) * 512 + 256 + (lane & 31) * 8);
        SPIN_END(__builtin_amdgcn_ballot_w64(umax2(umax2(p0.x, p0.y), umax2(p1.x, p1.y)) == SENT) == 0);
      }
      const float q0 = add_x32(asf(p0.x) + asf(p1.x)), q1 = add_x32(asf(p0.y) + asf(p1.y));
      v2u gr; gr.x = pkbf(q0, q1); gr.y = tag;
      st64(gr, rs, lane < 32 ? Q1_OFF + g * 8192 + part * 2048 + cb * 256 + lane * 8 : OOB);
      SCHED();
      *(volatile LAS u32*)(L + L_FL + 20) = 1u;
      STAMP(2);
    }
    if (w != 0) wait_flag(L, 5, 1);
    for (int i = 0; i < QDELAY; ++i) __builtin_amdgcn_s_sleep(4);
    const int q_off = Q1_OFF + g * 8192 + w * 2048 + lane * 32;
#define Q_ISSUE() v4u g0 = ld128(rs, q_off), g1 = ld128(rs, q_off + 16); SCHED(); bump_flag(L, 6, lane); STAMP(9)
#define QCNT()
#define Q_OK() (g0.y == tag && g0.w == tag && g1.y == tag && g1.w == tag)
#define Q_FINISH() \
    if (__builtin_amdgcn_ballot_w64(!Q_OK()) != 0) { \
      for (u32 spins = 0;;) { \
        CFENCE(); \
        if (!Q_OK()) { g0 = ld128(rs, q_off); g1 = ld128(rs, q_off + 16); } \
        QCNT(); \
        SPIN_END(__builtin_amdgcn_ballot_w64(!Q_OK()) == 0); \
      } \
    } \
    *(LAS v4u*)(L + L_QS + ((lane >> 5) * 4 + w) * 528 + (lane & 31) * 16) = v4u{g0.x, g0.z, g1.x, g1.z}; \
    CFENCE(); \
    *(volatile LAS u32*)(L + L_FL + w * 4) = 1u; \
    __builtin_amdgcn_s_setprio(0); \
    STAMP(3)
    if (is_o) {
      Q_ISSUE();
      Q_FINISH();
      SCHED();
      u32 xv = ldp<u32>(x + nc * 64 + (lane & 31) * 2);
      SCHED();
      v2u r[16];
      const u16* p = wo + (size_t)(h * 256 + w * 64 + kg * 16) * D + nc * 64 + m * 4;
#pragma unroll
      for (int i = 0; i < 16; ++i) r[i] = ldx<v2u, NT_TAIL>(p + (((i >> 2) & 1) * 8 + (i >> 3) * 4 + (i & 3)) * D);
      SCHED();
      wait_flag(L, 4, 1);
      wait_flag(L, 7, 4);
      for (int i = 0; i < ODELAY; ++i) __builtin_amdgcn_s_sleep(1);
      STAMP(5);
      __builtin_amdgcn_s_setprio(2);
      const int a_off = P2_OFF + (gs * 32 + m) * P2S + (h & 1) * 512 + w * 128 + kg * 16;
      v4u A[4];
      A[0] = ld128(rs, a_off); A[1] = ld128(rs, a_off + 64); A[2] = ld128(rs, a_off + 16 * P2S); A[3] = ld128(rs, a_off + 16 * P2S + 64);
      SCHED();
      xv = h == 0 ? xv : 0u;
      v4u B[2][4];
#pragma unroll
      for (int s = 0; s < 2; ++s)
#pragma unroll
        for (int ii = 0; ii < 4; ++ii) {
          const v2u lo = r[s * 8 + 2 * ii], hi = r[s * 8 + 2 * ii + 1];
          B[s][0][ii] = plo(hi.x, lo.x); B[s][1][ii] = phi(hi.x, lo.x); B[s][2][ii] = plo(hi.y, lo.y); B[s][3][ii] = phi(hi.y, lo.y);
        }
      asm volatile("" : "+v"(xv), "+v"(B[0][0]), "+v"(B[0][1]), "+v"(B[0][2]), "+v"(B[0][3]), "+v"(B[1][0]), "+v"(B[1][1]), "+v"(B[1][2]), "+v"(B[1][3]));
      STAMP(4);
      for (u32 spins = 0;;) {
        const bool ba = umax2(umax4(A[0]), umax4(A[1])) == SENT, bb = umax2(umax4(A[2]), umax4(A[3])) == SENT;
        SPIN_END(__builtin_amdgcn_ballot_w64(ba || bb) == 0);
        CFENCE();
        if (!SELP || ba) { A[0] = ld128(rs, a_off); A[1] = ld128(rs, a_off + 64); }
        if (!SELP || bb) { A[2] = ld128(rs, a_off + 16 * P2S); A[3] = ld128(rs, a_off + 16 * P2S + 64); }
      }
      STAMP(7);
      f32x4 C0[4], C1[4];
#pragma unroll
      for (int jj = 0; jj < 4; ++jj) { C0[jj] = mfma(A[0], B[0][jj], z); C1[jj] = mfma(A[2], B[0][jj], z); }
#pragma unroll
      for (int jj = 0; jj < 4; ++jj) { C0[jj] = mfma(A[1], B[1][jj], C0[jj]); C1[jj] = mfma(A[3], B[1][jj], C1[jj]); }
      v4u e0, e1;
      for (u32 spins = 0;;) {
        CFENCE();
        e0 = *(LAS v4u*)(L + L_WT + kg * 16); e1 = *(LAS v4u*)(L + L_WT + 64 + kg * 16);
        SPIN_END(__builtin_amdgcn_ballot_w64(umax2(umax4(e0), umax4(e1)) == SENT) == 0);
      }
      const float e[8] = {asf(e0.x), asf(e0.y), asf(e0.z), asf(e0.w), asf(e1.x), asf(e1.y), asf(e1.z), asf(e1.w)};
      float t[4];
#pragma unroll
      for (int jj = 0; jj < 4; ++jj) {
        const float t0 = e[0] * C0[jj][0] + e[1] * C0[jj][1], t1 = e[2] * C0[jj][2] + e[3] * C0[jj][3];
        const float t2 = e[4] * C1[jj][0] + e[5] * C1[jj][1], t3 = e[6] * C1[jj][2] + e[7] * C1[jj][3];
        t[jj] = (t0 + t1) + (t2 + t3);
      }
      auto r0 = __builtin_amdgcn_permlane32_swap(asu(t[0]), asu(t[2]), false, false);
      auto r1 = __builtin_amdgcn_permlane32_swap(asu(t[1]), asu(t[3]), false, false);
      auto r2 = __builtin_amdgcn_permlane16_swap(asu(asf(r0[0]) + asf(r0[1])), asu(asf(r1[0]) + asf(r1[1])), false, false);
      *(LAS float*)(L + L_RED + w * 256 + m * 16 + kg * 4) = asf(r2[0]) + asf(r2[1]);
      BAR();
      const LAS f32x2* rp = (const LAS f32x2*)(L + L_RED + (lane & 31) * 8);
      const f32x2 s = ((rp[0] + rp[32]) + (rp[64] + rp[96])) + f32x2{bflo(xv), bfhi(xv)};
      st32(nosent(pkbf(s[0], s[1])), rs, P3_OFF + (2 * w + (lane >> 5)) * P3R + h * P3S + nc * 128 + (lane & 31) * 4);
      STAMP(8);
    } else {
      Q_ISSUE();
      Q_FINISH();
    }
    if (is_f) {
      const int c2 = w * 128 + m * 8, ci = kg * 4 + (m & 3);
      v4u W2r[4];
      u32 b1r;
#define W2_LOAD() { \
        b1r = ldp<u16>(b1 + f0 + ci); \
        const u16* p2 = w2 + (size_t)(f0 + kg * 4) * D + (c2 < D ? c2 : 0); \
        _Pragma("unroll") for (int i = 0; i < 4; ++i) W2r[i] = ldx<v4u, NT_TAIL>(p2 + i * D); \
        SCHED(); }
      W2_LOAD();
      if (TAIL_GATE & 2) wait_flag(L, 7, 4);
      for (int i = 0; i < FDELAY; ++i) __builtin_amdgcn_s_sleep(4);
      SCHED();
      const int c = tid & 1, rq = tid >> 1, rqc = rq < 112 ? rq : 111;
      v4u W1r[4];
      {
        const u16* p = w1 + (size_t)(rqc * 4) * F + f0 + c * 8;
#pragma unroll
        for (int i = 0; i < 4; ++i) W1r[i] = ldx<v4u, NT_WQ>(p + ((i + ROT * f) & 3) * F);
        SCHED();
      }
      v2u B2[8];
#pragma unroll
      for (int jj = 0; jj < 8; ++jj) {
        B2[jj].x = (jj & 1) ? phi(W2r[1][jj >> 1], W2r[0][jj >> 1]) : plo(W2r[1][jj >> 1], W2r[0][jj >> 1]);
        B2[jj].y = (jj & 1) ? phi(W2r[3][jj >> 1], W2r[2][jj >> 1]) : plo(W2r[3][jj >> 1], W2r[2][jj >> 1]);
        asm volatile("" : "+v"(B2[jj]));
      }
      float b1v = bflo(b1r);
#pragma unroll
      for (int i = 0; i < 4; ++i) *(LAS v4u*)(L + L_W1 + (rq * 4 + ((i + ROT * f) & 3)) * 32 + c * 16) = rq < 112 ? W1r[i] : v4u{0u, 0u, 0u, 0u};
      LDSW();
      v4u Bf[4];
#pragma unroll
      for (int s = 0; s < 4; ++s) {
        LAS unsigned char* tp = L + L_W1 + ((4 * w + s) * 32 + kg * 8 + (m >> 2)) * 32 + (m & 3) * 8;
        const v2u lo = __builtin_bit_cast(v2u, __builtin_amdgcn_ds_read_tr16_b64_v4i16((LAS s16x4*)tp));
        const v2u hi = __builtin_bit_cast(v2u, __builtin_amdgcn_ds_read_tr16_b64_v4i16((LAS s16x4*)(tp + 128)));
        Bf[s] = v4u{lo.x, lo.y, hi.x, hi.y};
      }
      const v4u ones = {0x3F803F80u, 0x3F803F80u, 0x3F803F80u, 0x3F803F80u};
      f32x4 cs = z;
#pragma unroll
      for (int s = 0; s < 4; ++s) cs = mfma(ones, Bf[s], cs);
      float csv = cs[0];
      asm volatile("" : "+v"(csv), "+v"(b1v));
      STAMP(4);
      __builtin_amdgcn_s_setprio(2);
      wait_flag(L, 4, 1);
      STAMP(5);
      const int a_off = P3_OFF + rp3 * P3R + m * P3S + w * 256 + kg * 16;
      const int a_off2 = w < 3 ? a_off + 128 : OOB;
      v4u A[4] = {sent4, sent4, sent4, sent4};
      bool ba = true, bb = true;
      for (u32 spins = 0;;) {
        CFENCE();
        if (!SELP || ba) { A[0] = ld128(rs, a_off); A[1] = ld128(rs, a_off + 64); }
        if (!SELP || bb) { A[2] = ld128(rs, a_off2); A[3] = ld128(rs, a_off2 + 64); }
        ba = umax2(umax4(A[0]), umax4(A[1])) == SENT; bb = umax2(umax4(A[2]), umax4(A[3])) == SENT;
        SPIN_END(__builtin_amdgcn_ballot_w64(ba || bb) == 0);
      }
      STAMP(7);
      f32x4 U = z, G = z, S = z;
#pragma unroll
      for (int s = 0; s < 4; ++s) { G = mfma(A[s], A[s], G); S = mfma(A[s], ones, S); U = mfma(A[s], Bf[s], U); }
      f32x4 r;
      r[0] = add_x32(add_x16((U[0] + U[1]) + (U[2] + U[3])));
      r[1] = csv;
      r[2] = add_x32(add_x16((S[0] + S[1]) + (S[2] + S[3])));
      r[3] = wave_sum((G[0] + G[1]) + (G[2] + G[3]));
      if (lane < 16) *(LAS f32x4*)(L + L_RED + w * 256 + lane * 16) = r;
      BAR();
      const LAS f32x4* rp = (const LAS f32x4*)(L + L_RED + ci * 16);
      const f32x4 t = (rp[0] + rp[16]) + (rp[32] + rp[48]);
      const float mu2 = t[2] * (1.0f / D);
      const float var = fmaxf(t[3] * (1.0f / D) - mu2 * mu2, 0.f);
      const float rstd2 = __builtin_amdgcn_rsqf(var + 1e-5f);
      const float u = (t[0] - mu2 * t[1]) * rstd2 + b1v;
      const float n = __builtin_amdgcn_exp2f(-fabsf(u) * 1.4426950408889634f);
      const float num = u > 0.f ? 1.f + 2.f * n : n * n + 2.f * n;
      const float den = u > 0.f ? num + 2.f * n * n : num + 2.f;
      const float act = u * num * __builtin_amdgcn_rcpf(den);
      const u32 ap = pkbf(act, dpp<DPP_XOR1>(act));
      v2u a2; a2.x = asu(dpp<0x00>(asf(ap))); a2.y = asu(dpp<0xAA>(asf(ap)));
      f32x4 o2[8];
#pragma unroll
      for (int jj = 0; jj < 8; ++jj)
        o2[jj] = __builtin_amdgcn_mfma_f32_16x16x16bf16_1k(__builtin_bit_cast(s16x4, a2), __builtin_bit_cast(s16x4, B2[jj]), z, 0, 0, 0);
      v4u ov;
#pragma unroll
      for (int i = 0; i < 4; ++i) ov[i] = nosent(asu((kg & 1) ? o2[4 + i][0] : o2[i][0]));
      st128(ov, rs, (kg < 2 && c2 < D) ? P4_OFF + f * P4S + w * 512 + kg * 256 + m * 16 : OOB);
      STAMP(8);
    }
    FIN();
  }
}

extern "C" void kernel_launch(void* const* d_in, const int* in_sizes, int n_in, void* d_out, int out_size, void* d_ws,
                              size_t ws_size, hipStream_t stream) {
  fused_block<<<NWG, NTHR, 0, stream>>>((const u16*)d_in[0], (const u16*)d_in[1], (const u16*)d_in[2], (const u16*)d_in[3],
                                        (const u16*)d_in[4], (const u16*)d_in[6], (const u16*)d_in[7], (const u16*)d_in[8],
                                        (u16*)d_out, (unsigned char*)d_ws);
}
```

```cpp
#include <hip/hip_runtime.h>
#include <stdint.h>

#define GAS __attribute__((address_space(1)))
#define LAS __attribute__((address_space(3)))
typedef unsigned u32;
typedef unsigned short u16;
typedef u32 v4u __attribute__((ext_vector_type(4)));
typedef u32 v2u __attribute__((ext_vector_type(2)));
typedef float f32x4 __attribute__((ext_vector_type(4)));
typedef float f32x2 __attribute__((ext_vector_type(2)));
typedef short bf16x8 __attribute__((ext_vector_type(8)));
typedef short s16x4 __attribute__((ext_vector_type(4)));
typedef __bf16 bf16x2 __attribute__((ext_vector_type(2)));

#define NT_LOADS 1
#define NT_WQ 0
#define NT_TAIL 1
#define NK0 8
#ifndef NV0
#define NV0 2
#endif
#define TAIL_GATE 2
#define PRETOUCH 0
#define QDELAY 2
#define QSLEEP 0
#define WO_EARLY 0
#define ODELAY 0
#define KDELAY 2
#define CODEWARM 0
#define CODE_BYTES 10496
#define PDELAY 18
#define NKW 2
#define VPRE 0
#define FSTAMP 0
#define W2_EARLY 1
#define ROT 1
#define MAPB 1
#define GSH 1
#define RDELAY 4
#define FDELAY 0
#define SELP 1
#define STAMPS 0

constexpr int D = 448, DH = 256, SK = 8192, WIN = 2048, F = 1792, NQ = 4096;
constexpr int NWG = 256, NTHR = 576;
constexpr int AUX_SC1 = 16;
constexpr u32 SENT = 0xFFFFFFFFu;
constexpr u32 SPIN_CAP = 1u << 18;
constexpr int EPOCH_OFF = 0;
constexpr int RDY_OFF = 1024;
constexpr int Q1_OFF = 4096;
constexpr int S2_OFF = Q1_OFF + 65536;
constexpr int P2S = 1152, P3S = 896, P3R = 16 * P3S + 128, P4S = 1920;
constexpr int P2_OFF = 81920;
constexpr int P3_OFF = P2_OFF + 256 * P2S;
constexpr int P4_OFF = P3_OFF + 8 * P3R;
constexpr int WS_BYTES = P4_OFF + 112 * P4S;
constexpr int STAMP_OFF = 2 << 20;

__device__ __forceinline__ float asf(u32 u) { return __builtin_bit_cast(float, u); }
__device__ __forceinline__ u32 asu(float f) { return __builtin_bit_cast(u32, f); }
__device__ __forceinline__ float bflo(u32 w) { return asf(w << 16); }
__device__ __forceinline__ float bfhi(u32 w) { return asf(w & 0xFFFF0000u); }
__device__ __forceinline__ u32 pkbf(float lo, float hi) {
  bf16x2 r; r.x = (__bf16)lo; r.y = (__bf16)hi; return __builtin_bit_cast(u32, r);
}
__device__ __forceinline__ u32 nosent(u32 v) { return v < 0xFFFFFFFEu ? v : 0xFFFFFFFEu; }
template <int CTRL> __device__ __forceinline__ float dpp(float x) {
  return asf(__builtin_amdgcn_update_dpp(0, asu(x), CTRL, 0xF, 0xF, true));
}
#define DPP_XOR1 0xB1
#define DPP_XOR2 0x4E
#define DPP_ROR4 0x124
#define DPP_ROR8 0x128
__device__ __forceinline__ float add_x16(float x) {
  auto r = __builtin_amdgcn_permlane16_swap(asu(x), asu(x), false, false);
  return asf(r[0]) + asf(r[1]);
}
__device__ __forceinline__ float add_x32(float x) {
  auto r = __builtin_amdgcn_permlane32_swap(asu(x), asu(x), false, false);
  return asf(r[0]) + asf(r[1]);
}
__device__ __forceinline__ float max_x16(float x) {
  auto r = __builtin_amdgcn_permlane16_swap(asu(x), asu(x), false, false);
  return fmaxf(asf(r[0]), asf(r[1]));
}
__device__ __forceinline__ float max_x32(float x) {
  auto r = __builtin_amdgcn_permlane32_swap(asu(x), asu(x), false, false);
  return fmaxf(asf(r[0]), asf(r[1]));
}
__device__ __forceinline__ float row_sum16(float x) {
  x += dpp<DPP_XOR1>(x); x += dpp<DPP_XOR2>(x); x += dpp<DPP_ROR4>(x); x += dpp<DPP_ROR8>(x); return x;
}
__device__ __forceinline__ float wave_sum(float x) { return add_x32(add_x16(row_sum16(x))); }
__device__ __forceinline__ f32x4 mfma(v4u a, v4u b, f32x4 c) {
  return __builtin_amdgcn_mfma_f32_16x16x32_bf16(__builtin_bit_cast(bf16x8, a), __builtin_bit_cast(bf16x8, b), c, 0, 0, 0);
}
__device__ __forceinline__ float dot2(u32 a, u32 b, float c) {
  return __builtin_amdgcn_fdot2_f32_bf16(__builtin_bit_cast(bf16x2, a), __builtin_bit_cast(bf16x2, b), c, false);
}
__device__ __forceinline__ u32 plo(u32 hi, u32 lo) { return __builtin_amdgcn_perm(hi, lo, 0x05040100u); }
__device__ __forceinline__ u32 phi(u32 hi, u32 lo) { return __builtin_amdgcn_perm(hi, lo, 0x07060302u); }
template <typename T> __device__ __forceinline__ T ldg(const void* p) {
  return __builtin_nontemporal_load((const GAS T*)p);
}
template <typename T, int NT> __device__ __forceinline__ T ldx(const void* p) {
  if (NT) return __builtin_nontemporal_load((const GAS T*)p); else return *(const GAS T*)p;
}
template <typename T> __device__ __forceinline__ T ldp(const void* p) { return *(const GAS T*)p; }
#define BAR() do { __builtin_amdgcn_sched_barrier(0); asm volatile("s_waitcnt lgkmcnt(0)\n\ts_barrier" ::: "memory"); __builtin_amdgcn_sched_barrier(0); } while (0)
#define CFENCE() asm volatile("" ::: "memory")
#define SCHED() __builtin_amdgcn_sched_barrier(0)
__device__ __forceinline__ v4u ld128(__amdgpu_buffer_rsrc_t rs, int off) { return __builtin_amdgcn_raw_buffer_load_b128(rs, off, 0, AUX_SC1); }
__device__ __forceinline__ v2u ld64(__amdgpu_buffer_rsrc_t rs, int off) { return __builtin_amdgcn_raw_buffer_load_b64(rs, off, 0, AUX_SC1); }
__device__ __forceinline__ u32 ld32(__amdgpu_buffer_rsrc_t rs, int off) { return __builtin_amdgcn_raw_buffer_load_b32(rs, off, 0, AUX_SC1); }
__device__ __forceinline__ void st128(v4u v, __amdgpu_buffer_rsrc_t rs, int off) { __builtin_amdgcn_raw_buffer_store_b128(v, rs, off, 0, AUX_SC1); }
__device__ __forceinline__ void st64(v2u v, __amdgpu_buffer_rsrc_t rs, int off) { __builtin_amdgcn_raw_buffer_store_b64(v, rs, off, 0, AUX_SC1); }
__device__ __forceinline__ void st32(u32 v, __amdgpu_buffer_rsrc_t rs, int off) { __builtin_amdgcn_raw_buffer_store_b32(v, rs, off, 0, AUX_SC1); }
__device__ __forceinline__ u32 umax4(v4u v) { u32 a = v.x > v.y ? v.x : v.y, b = v.z > v.w ? v.z : v.w; return a > b ? a : b; }
__device__ __forceinline__ u32 umax2(u32 a, u32 b) { return a > b ? a : b; }

#define STAMP(i) SCHED()
#define STAMP_OUT()

constexpr int L_QP = 0;
constexpr int L_WT = 1024;
constexpr int L_SC = 2048;
constexpr int L_FL = 2560;
constexpr int L_RED = 2816;
constexpr int L_QS = 4096;
constexpr int L_W1 = 8448;
constexpr int L_BYTES = L_W1 + 16384;
constexpr int OOB = 0x7FFFFF00;
#define LDSW() asm volatile("s_waitcnt lgkmcnt(0)" ::: "memory")
#define SPIN_END(cond) if (cond) break; if (++spins > SPIN_CAP) break

__device__ __forceinline__ void wait_flag(LAS unsigned char* L, int idx, u32 n) {
  for (u32 spins = 0;;) {
    CFENCE();
    const u32 fl = *(volatile LAS u32*)(L + L_FL + idx * 4);
    SPIN_END(__builtin_amdgcn_readfirstlane(fl) >= n);
    __builtin_amdgcn_s_sleep(1);
  }
}
__device__ __forceinline__ void bump_flag(LAS unsigned char* L, int idx, int lane) {
  if (lane == 0) __hip_atomic_fetch_add((LAS u32*)(L + L_FL + idx * 4), 1u, __ATOMIC_RELAXED, __HIP_MEMORY_SCOPE_WORKGROUP);
}

typedef u32 v16u __attribute__((ext_vector_type(16)));
__device__ __forceinline__ const u16* mkptr(u32 lo, u32 hi) { return (const u16*)(((unsigned long long)hi << 32) | lo); }
#define ARGS() \
  v16u ka; v4u kb; \
  asm volatile("s_load_dwordx16 %0, %2, 0x0\n\ts_load_dwordx4 %1, %2, 0x40\n\ts_waitcnt lgkmcnt(0)" : "=&s"(ka), "=&s"(kb) : "s"(__builtin_amdgcn_kernarg_segment_ptr())); \
  const u16 *x = mkptr(ka[0], ka[1]), *Kc = mkptr(ka[2], ka[3]), *Vc = mkptr(ka[4], ka[5]), *wq = mkptr(ka[6], ka[7]); \
  const u16 *wo = mkptr(ka[8], ka[9]), *w1 = mkptr(ka[10], ka[11]), *b1 = mkptr(ka[12], ka[13]), *w2 = mkptr(ka[14], ka[15]); \
  u16* out = (u16*)mkptr(kb[0], kb[1]); \
  unsigned char* ws = (unsigned char*)mkptr(kb[2], kb[3]); \
  const __amdgpu_buffer_rsrc_t rs = __builtin_amdgcn_make_buffer_rsrc((void*)ws, 0, WS_BYTES, 0x00020000); \
  (void)x; (void)Kc; (void)Vc; (void)wq; (void)wo; (void)w1; (void)b1; (void)w2; (void)out
#define FIN() STAMP(10); STAMP_OUT(); __builtin_amdgcn_endpgm()

__global__ void __launch_bounds__(NTHR, 3)
fused_block(const u16*, const u16*, const u16*, const u16*, const u16*, const u16*, const u16*, const u16*, u16*, unsigned char*) {
  __shared__ __attribute__((aligned(16))) unsigned char smem[L_BYTES];
  LAS unsigned char* L = (LAS unsigned char*)smem;
  const int tid = threadIdx.x, lane = tid & 63, w = __builtin_amdgcn_readfirstlane(tid >> 6);
  const int b = blockIdx.x, g = b & 7, j = b >> 3;
  const bool is_o = j < 28 && g < 4, is_f = j < 28 && g >= 4;
  const int jo = j >= 14 ? j - 14 : j, gs = j >= 14 ? g + 4 : (g + 1) & 3;
  const int f = (g - 4) * 28 + j, rp3 = (g - 4) * 2 + (j & 1);
  const int h = 2 * gs + (jo >= 7 ? 1 : 0), nc = jo >= 7 ? jo - 7 : jo;
  const int f0 = f * 16;
  const int m = lane & 15, kg = lane >> 4;
  const f32x4 z = {0.f, 0.f, 0.f, 0.f};
  const v4u sent4 = {SENT, SENT, SENT, SENT};
  STAMP(0);
  if (__builtin_expect(w >= 8, 0)) {
    ARGS();
    const u32 ep = ld32(rs, EPOCH_OFF);
    st128(sent4, rs, P2_OFF + (g * 32 + j) * P2S + lane * 16);
    st128(sent4, rs, lane < 8 ? P2_OFF + (g * 32 + j) * P2S + 1024 + lane * 16 : OOB);
    st128(sent4, rs, is_o ? P3_OFF + (lane >> 3) * P3R + h * P3S + nc * 128 + (lane & 7) * 16 : is_f ? P4_OFF + f * P4S + lane * 16 : OOB);
    st128(sent4, rs, (is_f && lane < 56) ? P4_OFF + f * P4S + 1024 + lane * 16 : OOB);
    SCHED();
    BAR();
    asm volatile("s_waitcnt vmcnt(0)" ::: "memory");
    const u32 tag = __builtin_amdgcn_readfirstlane(ep) + 1u;
    st32(tag, rs, lane == 0 ? RDY_OFF + (g * 32 + j) * 4 : OOB);
    SCHED();
    STAMP(1);
    if (is_o || is_f) {
      wait_flag(L, is_o ? 6 : 3, 1);
      for (int i = 0; i < RDELAY; ++i) __builtin_amdgcn_s_sleep(4);
      const int rdy_off = RDY_OFF + (is_o ? gs * 128 + (lane & 7) * 16 : lane * 16);
      for (u32 spins = 0;;) {
        CFENCE();
        const v4u r = ld128(rs, rdy_off);
        SPIN_END(__builtin_amdgcn_ballot_w64(!(r.x == tag && r.y == tag && r.z == tag && r.w == tag)) == 0);
        __builtin_amdgcn_s_sleep(8);
      }
      CFENCE();
      *(volatile LAS u32*)(L + L_FL + 16) = 1u;
      STAMP(5);
    }
    if (is_o) {
      __builtin_amdgcn_s_setprio(2);
      wait_flag(L, 7, 4);
      for (int i = 0; i < ODELAY; ++i) __builtin_amdgcn_s_sleep(1);
      const int s_off = P2_OFF + (gs * 32 + (lane & 31)) * P2S + 1024 + (h & 1) * 64;
      u32 sv = SENT;
      for (u32 spins = 0;;) {
        CFENCE();
        if (sv == SENT && lane < 32) sv = ld32(rs, s_off);
        SPIN_END((u32)__builtin_amdgcn_ballot_w64(sv == SENT) == 0);
      }
      STAMP(6);
      float M = asf(sv);
      M = fmaxf(M, dpp<DPP_XOR1>(M)); M = fmaxf(M, dpp<DPP_XOR2>(M)); M = fmaxf(M, dpp<DPP_ROR4>(M)); M = fmaxf(M, dpp<DPP_ROR8>(M));
      M = max_x16(M);
      const float e = __builtin_amdgcn_exp2f(asf(sv) - M);
      const float Ls = add_x16(row_sum16(e));
      if (lane < 32) *(volatile LAS u32*)(L + L_WT + lane * 4) = nosent(asu(e * __builtin_amdgcn_rcpf(Ls)));
      STAMP(7);
    }
    if (is_f) {
      __builtin_amdgcn_s_setprio(2);
      const int fo = (f >> 5) * 512 + (f & 1) * 256 + ((f >> 1) & 15) * 16;
      const int o0 = P4_OFF + lane * P4S + fo, o1 = lane < 48 ? P4_OFF + (64 + lane) * P4S + fo : OOB;
      BAR();
      const v2u hp = ld64(rs, P3_OFF + rp3 * P3R + m * P3S + f * 8);
      if (PDELAY) __builtin_amdgcn_s_sleep(PDELAY);
      v4u r0 = sent4, r1 = sent4;
      bool ba = true, bb = true;
      for (u32 spins = 0;;) {
        CFENCE();
        if (!SELP || ba) r0 = ld128(rs, o0);
        if (!SELP || bb) r1 = ld128(rs, o1);
        ba = umax4(r0) == SENT; bb = umax4(r1) == SENT;
        SPIN_END(__builtin_amdgcn_ballot_w64(ba || bb) == 0);
      }
      STAMP(8);
      float v[4];
      v[0] = asf(r0.x) + (lane < 48 ? asf(r1.x) : bflo(hp.x));
      v[1] = asf(r0.y) + (lane < 48 ? asf(r1.y) : bfhi(hp.x));
      v[2] = asf(r0.z) + (lane < 48 ? asf(r1.z) : bflo(hp.y));
      v[3] = asf(r0.w) + (lane < 48 ? asf(r1.w) : bfhi(hp.y));
      auto t0 = __builtin_amdgcn_permlane32_swap(asu(v[0]), asu(v[2]), false, false);
      auto t1 = __builtin_amdgcn_permlane32_swap(asu(v[1]), asu(v[3]), false, false);
      const float u0 = row_sum16(add_x16(asf(t0[0]) + asf(t0[1]))), u1 = row_sum16(add_x16(asf(t1[0]) + asf(t1[1])));
      if ((lane & 31) == 0) {
        *(GAS u32*)(out + f * 4 + (lane >> 4)) = pkbf(u0, u1);
        if (f == 0 && lane == 0) st32(tag, rs, EPOCH_OFF);
      }
      STAMP(9);
    }
    FIN();
  }
  if (__builtin_expect(w >= 4, 0)) {
    const int a = w - 4;
    ARGS();
    const u16* Kp = Kc + ((size_t)g * SK + (SK - WIN) + j * 64 + a * 16 + m) * DH + kg * 8;
    const u16* Vp = Vc + ((size_t)g * SK + (SK - WIN) + j * 64 + (lane >> 3) * 8) * DH + a * 64 + (lane & 7) * 8;
    if (a < 3) *(LAS v4u*)(L + (tid - 256) * 16) = tid < 416 ? sent4 : v4u{0u, 0u, 0u, 0u};
    BAR();
    __builtin_amdgcn_s_setprio(1);
    v4u Kr[8], Vr[8];
#pragma unroll
    for (int i = 0; i < VPRE; ++i) Vr[i] = ldg<v4u>(Vp + i * DH);
    SCHED();
    if (a >= NKW) wait_flag(L, 5, 1);
    SCHED();
#pragma unroll
    for (int ks = 0; ks < 8; ++ks) Kr[ks] = ldg<v4u>(Kp + ks * 32);
    SCHED();
    if (a < NKW) wait_flag(L, 5, 1);
    SCHED();
#pragma unroll
    for (int i = VPRE; i < NV0; ++i) Vr[i] = ldg<v4u>(Vp + i * DH);
    SCHED();
    if (NV0 < 8) {
      wait_flag(L, 6, 4);
      SCHED();
#pragma unroll
      for (int i = NV0; i < 8; ++i) Vr[i] = ldg<v4u>(Vp + i * DH);
      SCHED();
    }
    bump_flag(L, 8, lane);
    STAMP(1);
    for (u32 spins = 0;;) {
      CFENCE();
      const v4u fl4 = *(volatile LAS v4u*)(L + L_FL);
      SPIN_END(__builtin_amdgcn_readfirstlane(fl4.x & fl4.y & fl4.z & fl4.w) != 0);
    }
    CFENCE();
    v4u qa[8];
#pragma unroll
    for (int ks = 0; ks < 8; ++ks) qa[ks] = *(LAS v4u*)(L + L_QS + (m & 7) * 528 + ks * 64 + kg * 16);
    STAMP(2);
    f32x4 acc = z, acc2 = z;
#pragma unroll
    for (int ks = 0; ks < 8; ks += 2) { acc = mfma(qa[ks], Kr[ks], acc); acc2 = mfma(qa[ks + 1], Kr[ks + 1], acc2); }
    {
      acc += acc2;
      float sv = (acc[0] + acc[1]) + (acc[2] + acc[3]);
      sv = bfhi(pkbf(0.f, sv)) * (0.0625f * 1.4426950408889634f);
      float mw = fmaxf(sv, dpp<DPP_XOR1>(sv));
      mw = fmaxf(mw, dpp<DPP_XOR2>(mw)); mw = fmaxf(mw, dpp<DPP_ROR4>(mw)); mw = fmaxf(mw, dpp<DPP_ROR8>(mw));
      const float p = __builtin_amdgcn_exp2f(sv - mw);
      if (lane < 32) *(volatile LAS u16*)(L + L_SC + (lane >> 4) * 128 + (a * 16 + m) * 2) = (u16)(pkbf(p, p) & 0x7FFFu);
      const float lw = row_sum16(p);
      if (lane < 32 && m < 2) *(volatile LAS u32*)(L + L_SC + 256 + (lane >> 4) * 32 + m * 16 + a * 4) = nosent(asu(m ? lw : mw));
    }
    STAMP(3);
    v4u B[8];
#pragma unroll
    for (int ii = 0; ii < 4; ++ii) {
#pragma unroll
      for (int jj = 0; jj < 8; ++jj)
        B[jj][ii] = (jj & 1) ? phi(Vr[2 * ii + 1][jj >> 1], Vr[2 * ii][jj >> 1]) : plo(Vr[2 * ii + 1][jj >> 1], Vr[2 * ii][jj >> 1]);
      SCHED();
    }
    STAMP(7);
    v4u pa;
    for (u32 spins = 0;;) {
      CFENCE();
      pa = *(LAS v4u*)(L + L_SC + (m >> 3) * 128 + (kg * 2 + ((m >> 2) & 1)) * 16);
      SPIN_END(__builtin_amdgcn_ballot_w64((((pa.x | pa.y) | (pa.z | pa.w)) & 0x80008000u) != 0) == 0);
    }
    v4u sm = *(LAS v4u*)(L + L_SC + 256 + (kg >> 1) * 32), sl = *(LAS v4u*)(L + L_SC + 256 + (kg >> 1) * 32 + 16);
    const u32 keep = (m & 3) == kg ? 0xFFFFFFFFu : 0u;
    pa &= keep;
    f32x4 c[8];
#pragma unroll
    for (int jj = 0; jj < 8; ++jj) c[jj] = mfma(pa, B[jj], z);
    for (u32 spins = 0;;) {
      SPIN_END(__builtin_amdgcn_ballot_w64(umax2(umax4(sm), umax4(sl)) == SENT) == 0);
      CFENCE();
      sm = *(LAS v4u*)(L + L_SC + 256 + (kg >> 1) * 32); sl = *(LAS v4u*)(L + L_SC + 256 + (kg >> 1) * 32 + 16);
    }
    STAMP(4);
    const float M = fmaxf(fmaxf(asf(sm.x), asf(sm.y)), fmaxf(asf(sm.z), asf(sm.w)));
    float fw[4];
#pragma unroll
    for (int i = 0; i < 4; ++i) fw[i] = __builtin_amdgcn_exp2f(asf(sm[i]) - M);
    const float Ls = (fw[0] * asf(sl.x) + fw[1] * asf(sl.y)) + (fw[2] * asf(sl.z) + fw[3] * asf(sl.w));
    const float inv = __builtin_amdgcn_rcpf(Ls);
    float o[8];
#pragma unroll
    for (int jj = 0; jj < 8; ++jj) o[jj] = (fw[0] * c[jj][0] + fw[1] * c[jj][1]) + (fw[2] * c[jj][2] + fw[3] * c[jj][3]);
    float os[4];
#pragma unroll
    for (int jj = 0; jj < 4; ++jj) {
      auto r = __builtin_amdgcn_permlane16_swap(asu(o[jj]), asu(o[jj + 4]), false, false);
      os[jj] = (asf(r[0]) + dpp<DPP_ROR8>(asf(r[1]))) * inv;
    }
    {
      v2u ov; ov.x = nosent(pkbf(os[0], os[1])); ov.y = nosent(pkbf(os[2], os[3]));
      st64(ov, rs, !(lane & 8) ? P2_OFF + (g * 32 + j) * P2S + (kg >> 1) * 512 + a * 128 + (kg & 1) * 64 + (lane & 7) * 8 : OOB);
      const float lse = M + __builtin_amdgcn_logf(Ls);
      st32(nosent(asu(lse)), rs, (a == 0 && !(kg & 1)) ? P2_OFF + (g * 32 + j) * P2S + 1024 + (kg >> 1) * 64 + m * 4 : OOB);
    }
    SCHED();
    bump_flag(L, 7, lane);
    STAMP(5);
    FIN();
  }
  {
    __builtin_amdgcn_s_setprio(3);
    const int part = j & 3, cb = j >> 2;
    const int qr = tid >> 3, qrow = part * 112 + (qr < 28 ? qr : 27) * 4;
    ARGS();
    const v4u xa = ldp<v4u>(x + (lane < 56 ? lane : 55) * 8);
    const v2u xq = ldp<v2u>(x + qrow);
    SCHED();
    v4u Wr[4];
    {
      const u16* p = wq + (size_t)qrow * NQ + g * 512 + cb * 64 + (lane & 7) * 8;
#pragma unroll
      for (int i = 0; i < 4; ++i) Wr[i] = ldx<v4u, NT_LOADS>(p + i * NQ);
    }
    const u32 ep = ld32(rs, EPOCH_OFF);
    SCHED();
    BAR();
    float s1 = ((bflo(xa[0]) + bfhi(xa[0])) + (bflo(xa[1]) + bfhi(xa[1]))) + ((bflo(xa[2]) + bfhi(xa[2])) + (bflo(xa[3]) + bfhi(xa[3])));
    s1 = lane < 56 ? s1 : 0.f;
    const float mu = wave_sum(s1) * (1.0f / D);
    float s2 = 0.f, s3 = 0.f;
#pragma unroll
    for (int i = 0; i < 4; ++i) { const float a = bflo(xa[i]) - mu, bb = bfhi(xa[i]) - mu; s2 += a * a; s3 += bb * bb; }
    s2 = lane < 56 ? s2 + s3 : 0.f;
    float rstd = __builtin_amdgcn_rsqf(wave_sum(s2) * (1.0f / D) + 1e-5f);
    rstd = qr < 28 ? rstd : 0.f;
    const u32 xn0 = pkbf((bflo(xq.x) - mu) * rstd, (bfhi(xq.x) - mu) * rstd);
    const u32 xn1 = pkbf((bflo(xq.y) - mu) * rstd, (bfhi(xq.y) - mu) * rstd);
    STAMP(11);
    float a[8];
#pragma unroll
    for (int jj = 0; jj < 4; ++jj) {
      a[2 * jj] = dot2(plo(Wr[3][jj], Wr[2][jj]), xn1, dot2(plo(Wr[1][jj], Wr[0][jj]), xn0, 0.f));
      a[2 * jj + 1] = dot2(phi(Wr[3][jj], Wr[2][jj]), xn1, dot2(phi(Wr[1][jj], Wr[0][jj]), xn0, 0.f));
    }
    float q2[2];
    {
      float t[4];
#pragma unroll
      for (int jj = 0; jj < 4; ++jj) {
        auto r = __builtin_amdgcn_permlane32_swap(asu(a[jj]), asu(a[jj + 4]), false, false);
        t[jj] = asf(r[0]) + asf(r[1]);
      }
#pragma unroll
      for (int jj = 0; jj < 2; ++jj) {
        auto r = __builtin_amdgcn_permlane16_swap(asu(t[jj]), asu(t[jj + 2]), false, false);
        q2[jj] = asf(r[0]) + asf(r[1]);
        q2[jj] += dpp<DPP_ROR8>(q2[jj]);
      }
    }
    if (!(lane & 8)) *(LAS v2u*)(L + L_QP + w * 256 + (lane & 7) * 32 + kg * 8) = v2u{nosent(asu(q2[0])), nosent(asu(q2[1]))};
    const u32 tag = __builtin_amdgcn_readfirstlane(ep) + 1u;
    STAMP(1);
    if (w == 0) {
      v2u p0, p1;
      for (u32 spins = 0;;) {
        CFENCE();
        p0 = *(LAS v2u*)(L + L_QP + (lane >> 5) * 512 + (lane & 31) * 8); p1 = *(LAS v2u*)(L + L_QP + (lane >> 5) * 512 + 256 + (lane & 31) * 8);
        SPIN_END(__builtin_amdgcn_ballot_w64(umax2(umax2(p0.x, p0.y), umax2(p1.x, p1.y)) == SENT) == 0);
      }
      const float q0 = add_x32(asf(p0.x) + asf(p1.x)), q1 = add_x32(asf(p0.y) + asf(p1.y));
      v2u gr; gr.x = pkbf(q0, q1); gr.y = tag;
      st64(gr, rs, lane < 32 ? Q1_OFF + g * 8192 + part * 2048 + cb * 256 + lane * 8 : OOB);
      SCHED();
      *(volatile LAS u32*)(L + L_FL + 20) = 1u;
      STAMP(2);
    }
    if (w != 0) wait_flag(L, 5, 1);
    for (int i = 0; i < QDELAY; ++i) __builtin_amdgcn_s_sleep(4);
    const int q_off = Q1_OFF + g * 8192 + w * 2048 + lane * 32;
#define Q_ISSUE() v4u g0 = ld128(rs, q_off), g1 = ld128(rs, q_off + 16); SCHED(); bump_flag(L, 6, lane); STAMP(9)
#define QCNT()
#define Q_OK() (g0.y == tag && g0.w == tag && g1.y == tag && g1.w == tag)
#define Q_FINISH() \
    if (__builtin_amdgcn_ballot_w64(!Q_OK()) != 0) { \
      for (u32 spins = 0;;) { \
        CFENCE(); \
        if (!Q_OK()) { g0 = ld128(rs, q_off); g1 = ld128(rs, q_off + 16); } \
        QCNT(); \
        SPIN_END(__builtin_amdgcn_ballot_w64(!Q_OK()) == 0); \
      } \
    } \
    *(LAS v4u*)(L + L_QS + ((lane >> 5) * 4 + w) * 528 + (lane & 31) * 16) = v4u{g0.x, g0.z, g1.x, g1.z}; \
    CFENCE(); \
    *(volatile LAS u32*)(L + L_FL + w * 4) = 1u; \
    __builtin_amdgcn_s_setprio(0); \
    STAMP(3)
    if (is_o) {
      Q_ISSUE();
      Q_FINISH();
      SCHED();
      u32 xv = ldp<u32>(x + nc * 64 + (lane & 31) * 2);
      SCHED();
      v2u r[16];
      const u16* p = wo + (size_t)(h * 256 + w * 64 + kg * 16) * D + nc * 64 + m * 4;
#pragma unroll
      for (int i = 0; i < 16; ++i) r[i] = ldx<v2u, NT_TAIL>(p + (((i >> 2) & 1) * 8 + (i >> 3) * 4 + (i & 3)) * D);
      SCHED();
      wait_flag(L, 4, 1);
      wait_flag(L, 7, 4);
      for (int i = 0; i < ODELAY; ++i) __builtin_amdgcn_s_sleep(1);
      STAMP(5);
      __builtin_amdgcn_s_setprio(2);
      const int a_off = P2_OFF + (gs * 32 + m) * P2S + (h & 1) * 512 + w * 128 + kg * 16;
      v4u A[4];
      A[0] = ld128(rs, a_off); A[1] = ld128(rs, a_off + 64); A[2] = ld128(rs, a_off + 16 * P2S); A[3] = ld128(rs, a_off + 16 * P2S + 64);
      SCHED();
      xv = h == 0 ? xv : 0u;
      v4u B[2][4];
#pragma unroll
      for (int s = 0; s < 2; ++s)
#pragma unroll
        for (int ii = 0; ii < 4; ++ii) {
          const v2u lo = r[s * 8 + 2 * ii], hi = r[s * 8 + 2 * ii + 1];
          B[s][0][ii] = plo(hi.x, lo.x); B[s][1][ii] = phi(hi.x, lo.x); B[s][2][ii] = plo(hi.y, lo.y); B[s][3][ii] = phi(hi.y, lo.y);
        }
      asm volatile("" : "+v"(xv), "+v"(B[0][0]), "+v"(B[0][1]), "+v"(B[0][2]), "+v"(B[0][3]), "+v"(B[1][0]), "+v"(B[1][1]), "+v"(B[1][2]), "+v"(B[1][3]));
      STAMP(4);
      for (u32 spins = 0;;) {
        const bool ba = umax2(umax4(A[0]), umax4(A[1])) == SENT, bb = umax2(umax4(A[2]), umax4(A[3])) == SENT;
        SPIN_END(__builtin_amdgcn_ballot_w64(ba || bb) == 0);
        CFENCE();
        if (!SELP || ba) { A[0] = ld128(rs, a_off); A[1] = ld128(rs, a_off + 64); }
        if (!SELP || bb) { A[2] = ld128(rs, a_off + 16 * P2S); A[3] = ld128(rs, a_off + 16 * P2S + 64); }
      }
      STAMP(7);
      f32x4 C0[4], C1[4];
#pragma unroll
      for (int jj = 0; jj < 4; ++jj) { C0[jj] = mfma(A[0], B[0][jj], z); C1[jj] = mfma(A[2], B[0][jj], z); }
#pragma unroll
      for (int jj = 0; jj < 4; ++jj) { C0[jj] = mfma(A[1], B[1][jj], C0[jj]); C1[jj] = mfma(A[3], B[1][jj], C1[jj]); }
      v4u e0, e1;
      for (u32 spins = 0;;) {
        CFENCE();
        e0 = *(LAS v4u*)(L + L_WT + kg * 16); e1 = *(LAS v4u*)(L + L_WT + 64 + kg * 16);
        SPIN_END(__builtin_amdgcn_ballot_w64(umax2(umax4(e0), umax4(e1)) == SENT) == 0);
      }
      const float e[8] = {asf(e0.x), asf(e0.y), asf(e0.z), asf(e0.w), asf(e1.x), asf(e1.y), asf(e1.z), asf(e1.w)};
      float t[4];
#pragma unroll
      for (int jj = 0; jj < 4; ++jj) {
        const float t0 = e[0] * C0[jj][0] + e[1] * C0[jj][1], t1 = e[2] * C0[jj][2] + e[3] * C0[jj][3];
        const float t2 = e[4] * C1[jj][0] + e[5] * C1[jj][1], t3 = e[6] * C1[jj][2] + e[7] * C1[jj][3];
        t[jj] = (t0 + t1) + (t2 + t3);
      }
      auto r0 = __builtin_amdgcn_permlane32_swap(asu(t[0]), asu(t[2]), false, false);
      auto r1 = __builtin_amdgcn_permlane32_swap(asu(t[1]), asu(t[3]), false, false);
      auto r2 = __builtin_amdgcn_permlane16_swap(asu(asf(r0[0]) + asf(r0[1])), asu(asf(r1[0]) + asf(r1[1])), false, false);
      *(LAS float*)(L + L_RED + w * 256 + m * 16 + kg * 4) = asf(r2[0]) + asf(r2[1]);
      BAR();
      const LAS f32x2* rp = (const LAS f32x2*)(L + L_RED + (lane & 31) * 8);
      const f32x2 s = ((rp[0] + rp[32]) + (rp[64] + rp[96])) + f32x2{bflo(xv), bfhi(xv)};
      st32(nosent(pkbf(s[0], s[1])), rs, P3_OFF + (2 * w + (lane >> 5)) * P3R + h * P3S + nc * 128 + (lane & 31) * 4);
      STAMP(8);
    } else {
      Q_ISSUE();
      Q_FINISH();
    }
    if (is_f) {
      const int c2 = w * 128 + m * 8, ci = kg * 4 + (m & 3);
      v4u W2r[4];
      u32 b1r;
#define W2_LOAD() { \
        b1r = ldp<u16>(b1 + f0 + ci); \
        const u16* p2 = w2 + (size_t)(f0 + kg * 4) * D + (c2 < D ? c2 : 0); \
        _Pragma("unroll") for (int i = 0; i < 4; ++i) W2r[i] = ldx<v4u, NT_TAIL>(p2 + i * D); \
        SCHED(); }
      W2_LOAD();
      if (TAIL_GATE & 2) wait_flag(L, 7, 4);
      for (int i = 0; i < FDELAY; ++i) __builtin_amdgcn_s_sleep(4);
      SCHED();
      const int c = tid & 1, rq = tid >> 1, rqc = rq < 112 ? rq : 111;
      v4u W1r[4];
      {
        const u16* p = w1 + (size_t)(rqc * 4) * F + f0 + c * 8;
#pragma unroll
        for (int i = 0; i < 4; ++i) W1r[i] = ldx<v4u, NT_WQ>(p + ((i + ROT * f) & 3) * F);
        SCHED();
      }
      v2u B2[8];
#pragma unroll
      for (int jj = 0; jj < 8; ++jj) {
        B2[jj].x = (jj & 1) ? phi(W2r[1][jj >> 1], W2r[0][jj >> 1]) : plo(W2r[1][jj >> 1], W2r[0][jj >> 1]);
        B2[jj].y = (jj & 1) ? phi(W2r[3][jj >> 1], W2r[2][jj >> 1]) : plo(W2r[3][jj >> 1], W2r[2][jj >> 1]);
        asm volatile("" : "+v"(B2[jj]));
      }
      float b1v = bflo(b1r);
#pragma unroll
      for (int i = 0; i < 4; ++i) *(LAS v4u*)(L + L_W1 + (rq * 4 + ((i + ROT * f) & 3)) * 32 + c * 16) = rq < 112 ? W1r[i] : v4u{0u, 0u, 0u, 0u};
      LDSW();
      v4u Bf[4];
#pragma unroll
      for (int s = 0; s < 4; ++s) {
        LAS unsigned char* tp = L + L_W1 + ((4 * w + s) * 32 + kg * 8 + (m >> 2)) * 32 + (m & 3) * 8;
        const v2u lo = __builtin_bit_cast(v2u, __builtin_amdgcn_ds_read_tr16_b64_v4i16((LAS s16x4*)tp));
        const v2u hi = __builtin_bit_cast(v2u, __builtin_amdgcn_ds_read_tr16_b64_v4i16((LAS s16x4*)(tp + 128)));
        Bf[s] = v4u{lo.x, lo.y, hi.x, hi.y};
      }
      const v4u ones = {0x3F803F80u, 0x3F803F80u, 0x3F803F80u, 0x3F803F80u};
      f32x4 cs = z;
#pragma unroll
      for (int s = 0; s < 4; ++s) cs = mfma(ones, Bf[s], cs);
      float csv = cs[0];
      asm volatile("" : "+v"(csv), "+v"(b1v));
      STAMP(4);
      __builtin_amdgcn_s_setprio(2);
      wait_flag(L, 4, 1);
      STAMP(5);
      const int a_off = P3_OFF + rp3 * P3R + m * P3S + w * 256 + kg * 16;
      const int a_off2 = w < 3 ? a_off + 128 : OOB;
      v4u A[4] = {sent4, sent4, sent4, sent4};
      bool ba = true, bb = true;
      for (u32 spins = 0;;) {
        CFENCE();
        if (!SELP || ba) { A[0] = ld128(rs, a_off); A[1] = ld128(rs, a_off + 64); }
        if (!SELP || bb) { A[2] = ld128(rs, a_off2); A[3] = ld128(rs, a_off2 + 64); }
        ba = umax2(umax4(A[0]), umax4(A[1])) == SENT; bb = umax2(umax4(A[2]), umax4(A[3])) == SENT;
        SPIN_END(__builtin_amdgcn_ballot_w64(ba || bb) == 0);
      }
      STAMP(7);
      f32x4 U = z, G = z, S = z;
#pragma unroll
      for (int s = 0; s < 4; ++s) { G = mfma(A[s], A[s], G); S = mfma(A[s], ones, S); U = mfma(A[s], Bf[s], U); }
      f32x4 r;
      r[0] = add_x32(add_x16((U[0] + U[1]) + (U[2] + U[3])));
      r[1] = csv;
      r[2] = add_x32(add_x16((S[0] + S[1]) + (S[2] + S[3])));
      r[3] = wave_sum((G[0] + G[1]) + (G[2] + G[3]));
      if (lane < 16) *(LAS f32x4*)(L + L_RED + w * 256 + lane * 16) = r;
      BAR();
      const LAS f32x4* rp = (const LAS f32x4*)(L + L_RED + ci * 16);
      const f32x4 t = (rp[0] + rp[16]) + (rp[32] + rp[48]);
      const float mu2 = t[2] * (1.0f / D);
      const float var = fmaxf(t[3] * (1.0f / D) - mu2 * mu2, 0.f);
      const float rstd2 = __builtin_amdgcn_rsqf(var + 1e-5f);
      const float u = (t[0] - mu2 * t[1]) * rstd2 + b1v;
      const float n = __builtin_amdgcn_exp2f(-fabsf(u) * 1.4426950408889634f);
      const float num = u > 0.f ? 1.f + 2.f * n : n * n + 2.f * n;
      const float den = u > 0.f ? num + 2.f * n * n : num + 2.f;
      const float act = u * num * __builtin_amdgcn_rcpf(den);
      const u32 ap = pkbf(act, dpp<DPP_XOR1>(act));
      v2u a2; a2.x = asu(dpp<0x00>(asf(ap))); a2.y = asu(dpp<0xAA>(asf(ap)));
      f32x4 o2[8];
#pragma unroll
      for (int jj = 0; jj < 8; ++jj)
        o2[jj] = __builtin_amdgcn_mfma_f32_16x16x16bf16_1k(__builtin_bit_cast(s16x4, a2), __builtin_bit_cast(s16x4, B2[jj]), z, 0, 0, 0);
      v4u ov;
#pragma unroll
      for (int i = 0; i < 4; ++i) ov[i] = nosent(asu((kg & 1) ? o2[4 + i][0] : o2[i][0]));
      st128(ov, rs, (kg < 2 && c2 < D) ? P4_OFF + f * P4S + w * 512 + kg * 256 + m * 16 : OOB);
      STAMP(8);
    }
    FIN();
  }
}

extern "C" void kernel_launch(void* const* d_in, const int* in_sizes, int n_in, void* d_out, int out_size, void* d_ws,
                              size_t ws_size, hipStream_t stream) {
  fused_block<<<NWG, NTHR, 0, stream>>>((const u16*)d_in[0], (const u16*)d_in[1], (const u16*)d_in[2], (const u16*)d_in[3],
                                        (const u16*)d_in[4], (const u16*)d_in[6], (const u16*)d_in[7], (const u16*)d_in[8],
                                        (u16*)d_out, (unsigned char*)d_ws);
}
```

```cpp
#include <hip/hip_runtime.h>
#include <stdint.h>

#define GAS __attribute__((address_space(1)))
#define LAS __attribute__((address_space(3)))
typedef unsigned u32;
typedef unsigned short u16;
typedef u32 v4u __attribute__((ext_vector_type(4)));
typedef u32 v2u __attribute__((ext_vector_type(2)));
typedef float f32x4 __attribute__((ext_vector_type(4)));
typedef float f32x2 __attribute__((ext_vector_type(2)));
typedef short bf16x8 __attribute__((ext_vector_type(8)));
typedef short s16x4 __attribute__((ext_vector_type(4)));
typedef __bf16 bf16x2 __attribute__((ext_vector_type(2)));

#define NT_LOADS 1
#define NT_WQ 0
#define NT_TAIL 1
#define NK0 8
#ifndef NV0
#define NV0 2
#endif
#define TAIL_GATE 2
#define PRETOUCH 0
#define QDELAY 3
#define QSLEEP 0
#define WO_EARLY 0
#define ODELAY 0
#define KDELAY 2
#define CODEWARM 1
#define CODE_BYTES 11008
#define PDELAY 18
#define NKW 2
#define VPRE 0
#define FSTAMP 0
#define W2_EARLY 1
#define ROT 1
#define MAPB 1
#define GSH 1
#define RDELAY 0
#define FDELAY 0
#define SELP 1
#define PREAD 3
#define STAMPS 0

constexpr int D = 448, DH = 256, SK = 8192, WIN = 2048, F = 1792, NQ = 4096;
constexpr int NWG = 256, NTHR = 576;
constexpr int AUX_SC1 = 16;
constexpr u32 SENT = 0xFFFFFFFFu;
constexpr u32 SPIN_CAP = 1u << 18;
constexpr int EPOCH_OFF = 0;
constexpr int RDY_OFF = 1024;
constexpr int Q1_OFF = 4096;
constexpr int S2_OFF = Q1_OFF + 65536;
constexpr int P2S = 1152, P3S = 896, P3R = 16 * P3S + 128, P4S = 1920;
constexpr int P2_OFF = 81920;
constexpr int P3_OFF = P2_OFF + 256 * P2S;
constexpr int P4_OFF = P3_OFF + 8 * P3R;
constexpr int WS_BYTES = P4_OFF + 112 * P4S;
constexpr int STAMP_OFF = 2 << 20;

__device__ __forceinline__ float asf(u32 u) { return __builtin_bit_cast(float, u); }
__device__ __forceinline__ u32 asu(float f) { return __builtin_bit_cast(u32, f); }
__device__ __forceinline__ float bflo(u32 w) { return asf(w << 16); }
__device__ __forceinline__ float bfhi(u32 w) { return asf(w & 0xFFFF0000u); }
__device__ __forceinline__ u32 pkbf(float lo, float hi) {
  bf16x2 r; r.x = (__bf16)lo; r.y = (__bf16)hi; return __builtin_bit_cast(u32, r);
}
__device__ __forceinline__ u32 nosent(u32 v) { return v < 0xFFFFFFFEu ? v : 0xFFFFFFFEu; }
template <int CTRL> __device__ __forceinline__ float dpp(float x) {
  return asf(__builtin_amdgcn_update_dpp(0, asu(x), CTRL, 0xF, 0xF, true));
}
#define DPP_XOR1 0xB1
#define DPP_XOR2 0x4E
#define DPP_ROR4 0x124
#define DPP_ROR8 0x128
__device__ __forceinline__ float add_x16(float x) {
  auto r = __builtin_amdgcn_permlane16_swap(asu(x), asu(x), false, false);
  return asf(r[0]) + asf(r[1]);
}
__device__ __forceinline__ float add_x32(float x) {
  auto r = __builtin_amdgcn_permlane32_swap(asu(x), asu(x), false, false);
  return asf(r[0]) + asf(r[1]);
}
__device__ __forceinline__ float max_x16(float x) {
  auto r = __builtin_amdgcn_permlane16_swap(asu(x), asu(x), false, false);
  return fmaxf(asf(r[0]), asf(r[1]));
}
__device__ __forceinline__ float max_x32(float x) {
  auto r = __builtin_amdgcn_permlane32_swap(asu(x), asu(x), false, false);
  return fmaxf(asf(r[0]), asf(r[1]));
}
__device__ __forceinline__ float row_sum16(float x) {
  x += dpp<DPP_XOR1>(x); x += dpp<DPP_XOR2>(x); x += dpp<DPP_ROR4>(x); x += dpp<DPP_ROR8>(x); return x;
}
__device__ __forceinline__ float wave_sum(float x) { return add_x32(add_x16(row_sum16(x))); }
__device__ __forceinline__ f32x4 mfma(v4u a, v4u b, f32x4 c) {
  return __builtin_amdgcn_mfma_f32_16x16x32_bf16(__builtin_bit_cast(bf16x8, a), __builtin_bit_cast(bf16x8, b), c, 0, 0, 0);
}
__device__ __forceinline__ float dot2(u32 a, u32 b, float c) {
  return __builtin_amdgcn_fdot2_f32_bf16(__builtin_bit_cast(bf16x2, a), __builtin_bit_cast(bf16x2, b), c, false);
}
__device__ __forceinline__ u32 plo(u32 hi, u32 lo) { return __builtin_amdgcn_perm(hi, lo, 0x05040100u); }
__device__ __forceinline__ u32 phi(u32 hi, u32 lo) { return __builtin_amdgcn_perm(hi, lo, 0x07060302u); }
template <typename T> __device__ __forceinline__ T ldg(const void* p) {
  return __builtin_nontemporal_load((const GAS T*)p);
}
template <typename T, int NT> __device__ __forceinline__ T ldx(const void* p) {
  if (NT) return __builtin_nontemporal_load((const GAS T*)p); else return *(const GAS T*)p;
}
template <typename T> __device__ __forceinline__ T ldp(const void* p) { return *(const GAS T*)p; }
#define BAR() do { __builtin_amdgcn_sched_barrier(0); asm volatile("s_waitcnt lgkmcnt(0)\n\ts_barrier" ::: "memory"); __builtin_amdgcn_sched_barrier(0); } while (0)
#define CFENCE() asm volatile("" ::: "memory")
#define SCHED() __builtin_amdgcn_sched_barrier(0)
__device__ __forceinline__ v4u ld128(__amdgpu_buffer_rsrc_t rs, int off) { return __builtin_amdgcn_raw_buffer_load_b128(rs, off, 0, AUX_SC1); }
__device__ __forceinline__ v2u ld64(__amdgpu_buffer_rsrc_t rs, int off) { return __builtin_amdgcn_raw_buffer_load_b64(rs, off, 0, AUX_SC1); }
__device__ __forceinline__ u32 ld32(__amdgpu_buffer_rsrc_t rs, int off) { return __builtin_amdgcn_raw_buffer_load_b32(rs, off, 0, AUX_SC1); }
__device__ __forceinline__ void st128(v4u v, __amdgpu_buffer_rsrc_t rs, int off) { __builtin_amdgcn_raw_buffer_store_b128(v, rs, off, 0, AUX_SC1); }
__device__ __forceinline__ void st64(v2u v, __amdgpu_buffer_rsrc_t rs, int off) { __builtin_amdgcn_raw_buffer_store_b64(v, rs, off, 0, AUX_SC1); }
__device__ __forceinline__ void st32(u32 v, __amdgpu_buffer_rsrc_t rs, int off) { __builtin_amdgcn_raw_buffer_store_b32(v, rs, off, 0, AUX_SC1); }
__device__ __forceinline__ u32 umax4(v4u v) { u32 a = v.x > v.y ? v.x : v.y, b = v.z > v.w ? v.z : v.w; return a > b ? a : b; }
__device__ __forceinline__ u32 umax2(u32 a, u32 b) { return a > b ? a : b; }

#define STAMP(i) SCHED()
#define STAMP_OUT()

constexpr int L_QP = 0;
constexpr int L_WT = 1024;
constexpr int L_SC = 2048;
constexpr int L_FL = 2560;
constexpr int L_RED = 2816;
constexpr int L_QS = 4096;
constexpr int L_W1 = 8448;
constexpr int L_BYTES = L_W1 + 16384;
constexpr int OOB = 0x7FFFFF00;
#define LDSW() asm volatile("s_waitcnt lgkmcnt(0)" ::: "memory")
#define SPIN_END(cond) if (cond) break; if (++spins > SPIN_CAP) break
#define SPIN_TOP() u32 thr = spins++ > SPIN_CAP ? SENT : SENT - 1; asm volatile("" : "+s"(thr) :: "memory")
#define ANY2(a, b) ((__builtin_amdgcn_ballot_w64(a) | __builtin_amdgcn_ballot_w64(b)) != 0)

__device__ __forceinline__ void wait_flag(LAS unsigned char* L, int idx, u32 n) {
  for (u32 spins = 0;;) {
    CFENCE();
    const u32 fl = *(volatile LAS u32*)(L + L_FL + idx * 4);
    SPIN_END(__builtin_amdgcn_readfirstlane(fl) >= n);
    __builtin_amdgcn_s_sleep(1);
  }
}
__device__ __forceinline__ void bump_flag(LAS unsigned char* L, int idx, int lane) {
  if (lane == 0) __hip_atomic_fetch_add((LAS u32*)(L + L_FL + idx * 4), 1u, __ATOMIC_RELAXED, __HIP_MEMORY_SCOPE_WORKGROUP);
}

typedef u32 v16u __attribute__((ext_vector_type(16)));
__device__ __forceinline__ const u16* mkptr(u32 lo, u32 hi) { return (const u16*)(((unsigned long long)hi << 32) | lo); }
#define ARGS() \
  v16u ka; v4u kb; \
  asm volatile("s_load_dwordx16 %0, %2, 0x0\n\ts_load_dwordx4 %1, %2, 0x40\n\ts_waitcnt lgkmcnt(0)" : "=&s"(ka), "=&s"(kb) : "s"(__builtin_amdgcn_kernarg_segment_ptr())); \
  const u16 *x = mkptr(ka[0], ka[1]), *Kc = mkptr(ka[2], ka[3]), *Vc = mkptr(ka[4], ka[5]), *wq = mkptr(ka[6], ka[7]); \
  const u16 *wo = mkptr(ka[8], ka[9]), *w1 = mkptr(ka[10], ka[11]), *b1 = mkptr(ka[12], ka[13]), *w2 = mkptr(ka[14], ka[15]); \
  u16* out = (u16*)mkptr(kb[0], kb[1]); \
  unsigned char* ws = (unsigned char*)mkptr(kb[2], kb[3]); \
  const __amdgpu_buffer_rsrc_t rs = __builtin_amdgcn_make_buffer_rsrc((void*)ws, 0, WS_BYTES, 0x00020000); \
  (void)x; (void)Kc; (void)Vc; (void)wq; (void)wo; (void)w1; (void)b1; (void)w2; (void)out
#define FIN() STAMP(10); STAMP_OUT(); __builtin_amdgcn_endpgm()

__global__ void __launch_bounds__(NTHR, 3)
fused_block(const u16*, const u16*, const u16*, const u16*, const u16*, const u16*, const u16*, const u16*, u16*, unsigned char*) {
  unsigned long long pc0;
  asm volatile("s_getpc_b64 %0" : "=s"(pc0));
  __shared__ __attribute__((aligned(16))) unsigned char smem[L_BYTES];
  LAS unsigned char* L = (LAS unsigned char*)smem;
  const int tid = threadIdx.x, lane = tid & 63, w = __builtin_amdgcn_readfirstlane(tid >> 6);
  const int b = blockIdx.x, g = b & 7, j = b >> 3;
  const bool is_o = j < 28 && g < 4, is_f = j < 28 && g >= 4;
  const int jo = j >= 14 ? j - 14 : j, gs = j >= 14 ? g + 4 : (g + 1) & 3;
  const int f = (g - 4) * 28 + j, rp3 = (g - 4) * 2 + (j & 1);
  const int h = 2 * gs + (jo >= 7 ? 1 : 0), nc = jo >= 7 ? jo - 7 : jo;
  const int f0 = f * 16;
  const int m = lane & 15, kg = lane >> 4;
  const f32x4 z = {0.f, 0.f, 0.f, 0.f};
  const v4u sent4 = {SENT, SENT, SENT, SENT};
  STAMP(0);
  if (__builtin_expect(w >= 8, 0)) {
    ARGS();
    const u32 ep = ld32(rs, EPOCH_OFF);
#define PT_ALL() \
    u32 pt = 0; \
    if (lane < 8) { \
      const int hr = is_o ? h * 256 : 0, fr = is_f ? f0 : 0; \
      const u16* pa = (lane & 1) ? (const u16*)out : Vc + ((size_t)g * SK + (SK - WIN) + j * 64) * DH; \
      const u16* pb = wo + (size_t)(hr + (lane & 1) * 255) * D; \
      const u16* pc = w1 + (size_t)((lane & 1) * 447) * F + fr; \
      const u16* pd = (lane & 1) ? b1 + fr : w2 + (size_t)fr * D; \
      pt = ldp<u32>((lane & 4) ? ((lane & 2) ? pd : pc) : ((lane & 2) ? pb : pa)); \
    } \
    SCHED()
    st128(sent4, rs, P2_OFF + (g * 32 + j) * P2S + lane * 16);
    st128(sent4, rs, lane < 8 ? P2_OFF + (g * 32 + j) * P2S + 1024 + lane * 16 : OOB);
    st128(sent4, rs, is_o ? P3_OFF + (lane >> 3) * P3R + h * P3S + nc * 128 + (lane & 7) * 16 : is_f ? P4_OFF + f * P4S + lane * 16 : OOB);
    st128(sent4, rs, (is_f && lane < 56) ? P4_OFF + f * P4S + 1024 + lane * 16 : OOB);
    SCHED();
    BAR();
    asm volatile("s_waitcnt vmcnt(0)" ::: "memory");
    const u32 tag = __builtin_amdgcn_readfirstlane(ep) + 1u;
    st32(tag, rs, lane == 0 ? RDY_OFF + (g * 32 + j) * 4 : OOB);
    SCHED();
    STAMP(1);
    if (j >= 28) {
      const unsigned char* cp = (const unsigned char*)(pc0 & ~255ull) + (j - 28) * (CODE_BYTES / 4) + (lane < CODE_BYTES / 256 ? lane : 0) * 64;
      const u32 cv = ldp<u32>(cp);
      asm volatile("" :: "v"(cv));
    }
    if (is_o || is_f) {
      wait_flag(L, is_o ? 6 : 3, 1);
      for (int i = 0; i < RDELAY; ++i) __builtin_amdgcn_s_sleep(4);
      const int rdy_off = RDY_OFF + (is_o ? gs * 128 + (lane & 7) * 16 : lane * 16);
      for (u32 spins = 0;;) {
        CFENCE();
        const v4u r = ld128(rs, rdy_off);
        SPIN_END(__builtin_amdgcn_ballot_w64(!(r.x == tag && r.y == tag && r.z == tag && r.w == tag)) == 0);
        __builtin_amdgcn_s_sleep(8);
      }
      CFENCE();
      *(volatile LAS u32*)(L + L_FL + 16) = 1u;
      STAMP(5);
    }
    if (is_o) {
      __builtin_amdgcn_s_setprio(2);
      wait_flag(L, 7, 4);
      for (int i = 0; i < ODELAY; ++i) __builtin_amdgcn_s_sleep(1);
      const int s_off = P2_OFF + (gs * 32 + (lane & 31)) * P2S + 1024 + (h & 1) * 64;
      u32 sv = SENT;
      for (u32 spins = 0;;) {
        CFENCE();
        if (sv == SENT && lane < 32) sv = ld32(rs, s_off);
        SPIN_END((u32)__builtin_amdgcn_ballot_w64(sv == SENT) == 0);
      }
      STAMP(6);
      float M = asf(sv);
      M = fmaxf(M, dpp<DPP_XOR1>(M)); M = fmaxf(M, dpp<DPP_XOR2>(M)); M = fmaxf(M, dpp<DPP_ROR4>(M)); M = fmaxf(M, dpp<DPP_ROR8>(M));
      M = max_x16(M);
      const float e = __builtin_amdgcn_exp2f(asf(sv) - M);
      const float Ls = add_x16(row_sum16(e));
      if (lane < 32) *(volatile LAS u32*)(L + L_WT + lane * 4) = nosent(asu(e * __builtin_amdgcn_rcpf(Ls)));
      STAMP(7);
    }
    if (is_f) {
      __builtin_amdgcn_s_setprio(2);
      const int fo = (f >> 5) * 512 + (f & 1) * 256 + ((f >> 1) & 15) * 16;
      const int o0 = P4_OFF + lane * P4S + fo, o1 = lane < 48 ? P4_OFF + (64 + lane) * P4S + fo : OOB;
      BAR();
      const v2u hp = ld64(rs, P3_OFF + rp3 * P3R + m * P3S + f * 8);
      if (PDELAY) __builtin_amdgcn_s_sleep(PDELAY);
      v4u r0 = sent4, r1 = {hp.x << 16, hp.x & 0xFFFF0000u, hp.y << 16, hp.y & 0xFFFF0000u};
      GAS u16* op = (GAS u16*)out + f * 4 + kg;
      asm volatile("" : "+v"(op), "+v"(r1));
      bool ba = true, bb = lane < 48;
      u32 spins = 0;
      float ta, tb;
      do {
        SPIN_TOP();
        if (ba) r0 = ld128(rs, o0);
        if (bb) r1 = ld128(rs, o1);
        SCHED();
        ba = umax4(r0) > thr; bb = umax4(r1) > thr;
        SCHED();
        const f32x4 v = __builtin_bit_cast(f32x4, r0) + __builtin_bit_cast(f32x4, r1);
        auto t0 = __builtin_amdgcn_permlane32_swap(asu(v[0]), asu(v[2]), false, false);
        auto t1 = __builtin_amdgcn_permlane32_swap(asu(v[1]), asu(v[3]), false, false);
        ta = asf(t0[0]) + asf(t0[1]); tb = asf(t1[0]) + asf(t1[1]);
      } while (ANY2(ba, bb));
      STAMP(8);
      auto t2 = __builtin_amdgcn_permlane16_swap(asu(ta), asu(tb), false, false);
      const float u0 = row_sum16(asf(t2[0]) + asf(t2[1]));
      if (m == 0) {
        *op = (u16)pkbf(u0, u0);
        if (f == 0 && lane == 0) st32(tag, rs, EPOCH_OFF);
      }
      STAMP(9);
    }
    FIN();
  }
  if (__builtin_expect(w >= 4, 0)) {
    const int a = w - 4;
    ARGS();
    const u16* Kp = Kc + ((size_t)g * SK + (SK - WIN) + j * 64 + a * 16 + m) * DH + kg * 8;
    const u16* Vp = Vc + ((size_t)g * SK + (SK - WIN) + j * 64 + (lane >> 3) * 8) * DH + a * 64 + (lane & 7) * 8;
    if (a < 3) *(LAS v4u*)(L + (tid - 256) * 16) = tid < 416 ? sent4 : v4u{0u, 0u, 0u, 0u};
    BAR();
    __builtin_amdgcn_s_setprio(1);
    v4u Kr[8], Vr[8];
#pragma unroll
    for (int i = 0; i < VPRE; ++i) Vr[i] = ldg<v4u>(Vp + i * DH);
    SCHED();
    if (a >= NKW) wait_flag(L, 5, 1);
    SCHED();
#pragma unroll
    for (int ks = 0; ks < 8; ++ks) Kr[ks] = ldg<v4u>(Kp + ks * 32);
    SCHED();
    if (a < NKW) wait_flag(L, 5, 1);
    SCHED();
#pragma unroll
    for (int i = VPRE; i < NV0; ++i) Vr[i] = ldg<v4u>(Vp + i * DH);
    SCHED();
    if (NV0 < 8) {
      wait_flag(L, 6, 4);
      SCHED();
#pragma unroll
      for (int i = NV0; i < 8; ++i) Vr[i] = ldg<v4u>(Vp + i * DH);
      SCHED();
    }
    bump_flag(L, 8, lane);
    STAMP(1);
    int qa_a = L_QS + (m & 7) * 528 + kg * 16, p_w = L_SC + (kg & 1) * 128 + (a * 16 + m) * 2, st_w = L_SC + 256 + (kg & 1) * 32 + a * 8;
    int pa_a = L_SC + (m >> 3) * 128 + (kg * 2 + ((m >> 2) & 1)) * 16, st_r = L_SC + 256 + (kg >> 1) * 32;
    int o_off = !(lane & 8) ? P2_OFF + (g * 32 + j) * P2S + (kg >> 1) * 512 + a * 128 + (kg & 1) * 64 + (lane & 7) * 8 : OOB;
    int l_off = (a == 0 && !(kg & 1)) ? P2_OFF + (g * 32 + j) * P2S + 1024 + (kg >> 1) * 64 + m * 4 : OOB;
    u32 keep = (m & 3) == kg ? 0xFFFFFFFFu : 0u;
    asm volatile("" : "+v"(qa_a), "+v"(p_w), "+v"(st_w), "+v"(pa_a), "+v"(st_r), "+v"(o_off), "+v"(l_off), "+v"(keep));
    for (u32 spins = 0;;) {
      CFENCE();
      const v4u fl4 = *(volatile LAS v4u*)(L + L_FL);
      SPIN_END(__builtin_amdgcn_readfirstlane(fl4.x & fl4.y & fl4.z & fl4.w) != 0);
    }
    CFENCE();
    v4u qa[8];
#pragma unroll
    for (int ks = 0; ks < 8; ++ks) qa[ks] = *(LAS v4u*)(L + qa_a + ks * 64);
    STAMP(2);
    f32x4 acc = z;
#pragma unroll
    for (int ks = 0; ks < 8; ++ks) acc = mfma(qa[ks], Kr[ks], acc);
    {
      const f32x2 a2 = acc.xy + acc.zw;
      float sv = a2.x + a2.y;
      sv = bflo(pkbf(sv, sv)) * (0.0625f * 1.4426950408889634f);
      float mw;
      asm volatile("s_nop 1\n\tv_max_f32_dpp %0, %1, %1 quad_perm:[1,0,3,2] row_mask:0xf bank_mask:0xf\n\t"
                   "s_nop 1\n\tv_max_f32_dpp %0, %0, %0 quad_perm:[2,3,0,1] row_mask:0xf bank_mask:0xf\n\t"
                   "s_nop 1\n\tv_max_f32_dpp %0, %0, %0 row_ror:4 row_mask:0xf bank_mask:0xf\n\t"
                   "s_nop 1\n\tv_max_f32_dpp %0, %0, %0 row_ror:8 row_mask:0xf bank_mask:0xf" : "=&v"(mw) : "v"(sv));
      const float p = __builtin_amdgcn_exp2f(sv - mw);
      *(volatile LAS u16*)(L + p_w) = (u16)(pkbf(p, p) & 0x7FFFu);
      const float lw = row_sum16(p);
      if (m == 0) *(volatile LAS v2u*)(L + st_w) = v2u{nosent(asu(mw)), nosent(asu(lw))};
    }
    STAMP(3);
    v4u B[8], pr, s0, s1;
#define P_READ() CFENCE(); pr = *(LAS v4u*)(L + pa_a); s0 = *(LAS v4u*)(L + st_r); s1 = *(LAS v4u*)(L + st_r + 16)
#pragma unroll
    for (int ii = 0; ii < 4; ++ii) {
      if (ii == PREAD) { P_READ(); SCHED(); }
#pragma unroll
      for (int jj = 0; jj < 8; ++jj)
        B[jj][ii] = (jj & 1) ? phi(Vr[2 * ii + 1][jj >> 1], Vr[2 * ii][jj >> 1]) : plo(Vr[2 * ii + 1][jj >> 1], Vr[2 * ii][jj >> 1]);
      SCHED();
    }
    asm volatile("" : "+v"(B[0]), "+v"(B[1]), "+v"(B[2]), "+v"(B[3]), "+v"(B[4]), "+v"(B[5]), "+v"(B[6]), "+v"(B[7]));
    STAMP(7);
    v4u pa;
    f32x4 c[8];
    {
      u32 spins = 0;
      bool bad;
      do {
        if (PREAD > 3 || spins) { P_READ(); }
        u32 mk = spins++ > SPIN_CAP ? 0u : 0x80008000u;
        asm volatile("" : "+s"(mk) :: "memory");
        SCHED();
        pa = pr & keep;
        c[0] = mfma(pa, B[0], z); SCHED();
        bad = (((pr.x | pr.y) | (pr.z | pr.w)) & mk) != 0; SCHED();
        c[4] = mfma(pa, B[4], z); SCHED();
      } while (__builtin_amdgcn_ballot_w64(bad) != 0);
    }
    float M, Ls;
    u32 lse;
    f32x4 fw;
#define STATS_A() M = fmaxf(fmaxf(asf(s0.x), asf(s0.z)), fmaxf(asf(s1.x), asf(s1.z))); \
    fw = f32x4{__builtin_amdgcn_exp2f(asf(s0.x) - M), __builtin_amdgcn_exp2f(asf(s0.z) - M), __builtin_amdgcn_exp2f(asf(s1.x) - M), __builtin_amdgcn_exp2f(asf(s1.z) - M)}
#define STATS_B() Ls = (fw[0] * asf(s0.y) + fw[1] * asf(s0.w)) + (fw[2] * asf(s1.y) + fw[3] * asf(s1.w)); fw *= __builtin_amdgcn_rcpf(Ls); \
    lse = nosent(asu(M + __builtin_amdgcn_logf(Ls)))
    c[1] = mfma(pa, B[1], z); SCHED();
    const bool sbad = umax2(umax4(s0), umax4(s1)) == SENT; SCHED();
    c[5] = mfma(pa, B[5], z); SCHED();
    STATS_A(); asm volatile("" : "+v"(fw), "+v"(M)); SCHED();
    c[2] = mfma(pa, B[2], z); c[6] = mfma(pa, B[6], z); SCHED();
    STATS_B(); asm volatile("" : "+v"(fw), "+v"(lse)); SCHED();
    c[3] = mfma(pa, B[3], z); c[7] = mfma(pa, B[7], z); SCHED();
    if (__builtin_expect(__builtin_amdgcn_ballot_w64(sbad) != 0, 0)) {
      for (u32 spins = 0;;) {
        CFENCE();
        s0 = *(LAS v4u*)(L + st_r); s1 = *(LAS v4u*)(L + st_r + 16);
        SPIN_END(__builtin_amdgcn_ballot_w64(umax2(umax4(s0), umax4(s1)) == SENT) == 0);
      }
      STATS_A(); STATS_B();
    }
    st32(lse, rs, l_off);
    STAMP(4);
    float os[4];
#pragma unroll
    for (int jj = 0; jj < 4; ++jj) {
      const f32x2 oa = __builtin_elementwise_fma(fw.zw, c[jj].zw, fw.xy * c[jj].xy), ob = __builtin_elementwise_fma(fw.zw, c[jj + 4].zw, fw.xy * c[jj + 4].xy);
      float o0 = oa.x + oa.y, o1 = ob.x + ob.y;
      asm volatile("" : "+v"(o0), "+v"(o1));
      auto r = __builtin_amdgcn_permlane16_swap(asu(o0), asu(o1), false, false);
      os[jj] = asf(r[0]) + dpp<DPP_ROR8>(asf(r[1]));
      asm volatile("" : "+v"(os[jj]));
    }
    {
      v2u ov; ov.x = nosent(pkbf(os[0], os[1])); ov.y = nosent(pkbf(os[2], os[3]));
      st64(ov, rs, o_off);
    }
    SCHED();
    bump_flag(L, 7, lane);
    STAMP(5);
    FIN();
  }
  {
    __builtin_amdgcn_s_setprio(3);
    const int part = j & 3, cb = j >> 2;
    const int qr = tid >> 3, qrow = part * 112 + (qr < 28 ? qr : 27) * 4;
    ARGS();
    const v4u xa = ldp<v4u>(x + (lane < 56 ? lane : 55) * 8);
    const v2u xq = ldp<v2u>(x + qrow);
    SCHED();
    v4u Wr[4];
    {
      const u16* p = wq + (size_t)qrow * NQ + g * 512 + cb * 64 + (lane & 7) * 8;
#pragma unroll
      for (int i = 0; i < 4; ++i) Wr[i] = ldx<v4u, NT_LOADS>(p + i * NQ);
    }
    const u32 ep = ld32(rs, EPOCH_OFF);
    SCHED();
    BAR();
    int qp_w = L_QP + w * 256 + (lane & 7) * 32 + kg * 8, qp_r = L_QP + (lane & 31) * 8;
    int pub_off = lane < 32 ? Q1_OFF + g * 8192 + part * 2048 + cb * 256 + lane * 8 : OOB;
    int q_off = Q1_OFF + g * 8192 + w * 2048 + lane * 32, qs_w = L_QS + ((lane >> 5) * 4 + w) * 528 + (lane & 31) * 16;
    float a[8] = {0.f, 0.f, 0.f, 0.f, 0.f, 0.f, 0.f, 0.f};
    asm volatile("" : "+v"(qp_w), "+v"(qp_r), "+v"(pub_off), "+v"(q_off), "+v"(qs_w), "+v"(a[0]), "+v"(a[1]), "+v"(a[2]), "+v"(a[3]), "+v"(a[4]), "+v"(a[5]), "+v"(a[6]), "+v"(a[7]));
    SCHED();
    float s1 = ((bflo(xa[0]) + bfhi(xa[0])) + (bflo(xa[1]) + bfhi(xa[1]))) + ((bflo(xa[2]) + bfhi(xa[2])) + (bflo(xa[3]) + bfhi(xa[3])));
    s1 = lane < 56 ? s1 : 0.f;
    const float mu = wave_sum(s1) * (1.0f / D);
    float s2 = 0.f, s3 = 0.f;
#pragma unroll
    for (int i = 0; i < 4; ++i) { const float aa = bflo(xa[i]) - mu, bb = bfhi(xa[i]) - mu; s2 += aa * aa; s3 += bb * bb; }
    s2 = lane < 56 ? s2 + s3 : 0.f;
    float rstd = __builtin_amdgcn_rsqf(wave_sum(s2) * (1.0f / D) + 1e-5f);
    rstd = qr < 28 ? rstd : 0.f;
    const u32 xn0 = pkbf((bflo(xq.x) - mu) * rstd, (bfhi(xq.x) - mu) * rstd);
    const u32 xn1 = pkbf((bflo(xq.y) - mu) * rstd, (bfhi(xq.y) - mu) * rstd);
    STAMP(11);
#pragma unroll
    for (int jj = 0; jj < 4; ++jj) {
      a[2 * jj] = dot2(plo(Wr[1][jj], Wr[0][jj]), xn0, a[2 * jj]);
      a[2 * jj + 1] = dot2(phi(Wr[1][jj], Wr[0][jj]), xn0, a[2 * jj + 1]);
    }
    SCHED();
#pragma unroll
    for (int jj = 0; jj < 4; ++jj) {
      const int k = (jj >> 1) | ((jj & 1) << 1);
      a[2 * k] = dot2(plo(Wr[3][k], Wr[2][k]), xn1, a[2 * k]);
      a[2 * k + 1] = dot2(phi(Wr[3][k], Wr[2][k]), xn1, a[2 * k + 1]);
    }
    float q2[2];
    {
      float t[4];
#pragma unroll
      for (int jj = 0; jj < 4; ++jj) {
        auto r = __builtin_amdgcn_permlane32_swap(asu(a[jj]), asu(a[jj + 4]), false, false);
        t[jj] = asf(r[0]) + asf(r[1]);
      }
#pragma unroll
      for (int jj = 0; jj < 2; ++jj) {
        auto r = __builtin_amdgcn_permlane16_swap(asu(t[jj]), asu(t[jj + 2]), false, false);
        q2[jj] = asf(r[0]) + asf(r[1]);
        asm volatile("" : "+v"(q2[jj]));
        q2[jj] += dpp<DPP_ROR8>(q2[jj]);
        asm volatile("" : "+v"(q2[jj]));
      }
    }
    *(LAS v2u*)(L + qp_w) = v2u{nosent(asu(q2[0])), nosent(asu(q2[1]))};
    SCHED();
    const u32 tag = __builtin_amdgcn_readfirstlane(ep) + 1u;
    STAMP(1);
    if (w == 0) {
      u32 spins = 0;
      bool bad;
      v2u gr; gr.y = tag;
      do {
        SPIN_TOP();
        const v2u p0 = *(LAS v2u*)(L + qp_r), p1 = *(LAS v2u*)(L + qp_r + 256), p2 = *(LAS v2u*)(L + qp_r + 512), p3 = *(LAS v2u*)(L + qp_r + 768);
        bad = umax2(umax4(v4u{p0.x, p0.y, p1.x, p1.y}), umax4(v4u{p2.x, p2.y, p3.x, p3.y})) > thr;
        const f32x2 qs = (__builtin_bit_cast(f32x2, p0) + __builtin_bit_cast(f32x2, p1)) + (__builtin_bit_cast(f32x2, p2) + __builtin_bit_cast(f32x2, p3));
        gr.x = pkbf(qs.x, qs.y);
        asm volatile("" : "+v"(gr));
      } while (__builtin_amdgcn_ballot_w64(bad) != 0);
      st64(gr, rs, pub_off);
      SCHED();
      *(volatile LAS u32*)(L + L_FL + 20) = 1u;
      STAMP(2);
    }
    if (w != 0) wait_flag(L, 5, 1);
    for (int i = 0; i < QDELAY; ++i) __builtin_amdgcn_s_sleep(4);
#define Q_ISSUE() v4u g0 = ld128(rs, q_off), g1 = ld128(rs, q_off + 16); SCHED(); bump_flag(L, 6, lane); STAMP(9)
#define QCNT()
#define Q_FINISH() \
    { \
      u32 spins = 0; \
      bool bad = false; \
      do { \
        u32 mk;     \
        asm volatile("s_cmp_gt_u32 %1, %2\n\ts_cselect_b32 %0, 0, -1" : "=s"(mk) : "s"(spins++), "s"(SPIN_CAP) : "scc", "memory"); \
        if (bad) { g0 = ld128(rs, q_off); g1 = ld128(rs, q_off + 16); } \
        *(LAS v4u*)(L + qs_w) = v4u{g0.x, g0.z, g1.x, g1.z};     \
        bad = ((((g0.y ^ tag) | (g0.w ^ tag)) | ((g1.y ^ tag) | (g1.w ^ tag))) & mk) != 0; \
      } while (__builtin_amdgcn_ballot_w64(bad) != 0); \
    } \
    CFENCE(); \
    *(volatile LAS u32*)(L + L_FL + w * 4) = 1u; \
    __builtin_amdgcn_s_setprio(0); \
    STAMP(3)
    if (is_o) {
      Q_ISSUE();
      Q_FINISH();
      SCHED();
      u32 xv = ldp<u32>(x + nc * 64 + (lane & 31) * 2);
      SCHED();
      v2u r[16];
      const u16* p = wo + (size_t)(h * 256 + w * 64 + kg * 16) * D + nc * 64 + m * 4;
#pragma unroll
      for (int i = 0; i < 16; ++i) r[i] = ldx<v2u, NT_TAIL>(p + (((i >> 2) & 1) * 8 + (i >> 3) * 4 + (i & 3)) * D);
      SCHED();
      wait_flag(L, 4, 1);
      wait_flag(L, 7, 4);
      for (int i = 0; i < ODELAY; ++i) __builtin_amdgcn_s_sleep(1);
      STAMP(5);
      __builtin_amdgcn_s_setprio(2);
      const int a_off = P2_OFF + (gs * 32 + m) * P2S + (h & 1) * 512 + w * 128 + kg * 16;
      v4u A[4];
      A[0] = ld128(rs, a_off); A[1] = ld128(rs, a_off + 64); A[2] = ld128(rs, a_off + 16 * P2S); A[3] = ld128(rs, a_off + 16 * P2S + 64);
      SCHED();
      xv = h == 0 ? xv : 0u;
      v4u B[2][4];
#pragma unroll
      for (int s = 0; s < 2; ++s)
#pragma unroll
        for (int ii = 0; ii < 4; ++ii) {
          const v2u lo = r[s * 8 + 2 * ii], hi = r[s * 8 + 2 * ii + 1];
          B[s][0][ii] = plo(hi.x, lo.x); B[s][1][ii] = phi(hi.x, lo.x); B[s][2][ii] = plo(hi.y, lo.y); B[s][3][ii] = phi(hi.y, lo.y);
        }
      asm volatile("" : "+v"(xv), "+v"(B[0][0]), "+v"(B[0][1]), "+v"(B[0][2]), "+v"(B[0][3]), "+v"(B[1][0]), "+v"(B[1][1]), "+v"(B[1][2]), "+v"(B[1][3]));
      int wt_a = L_WT + kg * 16, red_w = L_RED + w * 256 + m * 16 + kg * 4, red_r = L_RED + (lane & 31) * 8;
      int p3_off = P3_OFF + (2 * w + (lane >> 5)) * P3R + h * P3S + nc * 128 + (lane & 31) * 4;
      f32x2 xr = {bflo(xv), bfhi(xv)};
      asm volatile("" : "+v"(wt_a), "+v"(red_w), "+v"(red_r), "+v"(p3_off), "+v"(xr));
      STAMP(4);
      f32x4 C0[4], C1[4];
      bool ba = false, bb = false;
      u32 spins = 0;
      do {
        SPIN_TOP();
        if (ba) { A[0] = ld128(rs, a_off); A[1] = ld128(rs, a_off + 64); }
        if (bb) { A[2] = ld128(rs, a_off + 16 * P2S); A[3] = ld128(rs, a_off + 16 * P2S + 64); }
        SCHED();
        C0[0] = mfma(A[0], B[0][0], z); SCHED();
        const u32 ma = umax4(A[0]); SCHED();
        C0[1] = mfma(A[0], B[0][1], z); SCHED();
        ba = umax2(ma, umax4(A[1])) > thr; SCHED();
        C0[0] = mfma(A[1], B[1][0], C0[0]); SCHED();
        const u32 mb = umax4(A[2]); SCHED();
        C1[0] = mfma(A[2], B[0][0], z); SCHED();
        bb = umax2(mb, umax4(A[3])) > thr; SCHED();
        C0[1] = mfma(A[1], B[1][1], C0[1]); SCHED();
        C1[0] = mfma(A[3], B[1][0], C1[0]); SCHED();
      } while (ANY2(ba, bb));
      STAMP(7);
      C1[1] = mfma(A[2], B[0][1], z); C0[2] = mfma(A[0], B[0][2], z); SCHED();
      CFENCE();
      v4u e0 = *(LAS v4u*)(L + wt_a), e1 = *(LAS v4u*)(L + wt_a + 64);
      SCHED();
      C1[1] = mfma(A[3], B[1][1], C1[1]); C1[2] = mfma(A[2], B[0][2], z); C0[2] = mfma(A[1], B[1][2], C0[2]); C0[3] = mfma(A[0], B[0][3], z); SCHED();
      C1[2] = mfma(A[3], B[1][2], C1[2]); C1[3] = mfma(A[2], B[0][3], z); SCHED();
#define COMB(jj) { \
        const f32x4 ea = __builtin_bit_cast(f32x4, e0), eb = __builtin_bit_cast(f32x4, e1); \
        f32x2 c = ea.xy * C0[jj].xy, d = eb.xy * C1[jj].xy; \
        c = __builtin_elementwise_fma(ea.zw, C0[jj].zw, c); d = __builtin_elementwise_fma(eb.zw, C1[jj].zw, d); \
        c += d; t[jj] = c.x + c.y; }
      float t[4];
      const bool be = umax2(umax4(e0), umax4(e1)) == SENT; SCHED();
      C0[3] = mfma(A[1], B[1][3], C0[3]); SCHED();
      COMB(0); asm volatile("" : "+v"(t[0])); SCHED();
      C1[3] = mfma(A[3], B[1][3], C1[3]); SCHED();
      COMB(2); COMB(1); asm volatile("" : "+v"(t[1]), "+v"(t[2])); SCHED();
      if (__builtin_expect(__builtin_amdgcn_ballot_w64(be) != 0, 0)) {
        for (u32 spins = 0;;) {
          CFENCE();
          e0 = *(LAS v4u*)(L + wt_a); e1 = *(LAS v4u*)(L + wt_a + 64);
          SPIN_END(__builtin_amdgcn_ballot_w64(umax2(umax4(e0), umax4(e1)) == SENT) == 0);
        }
        COMB(0); COMB(2); COMB(1);
      }
      auto r0 = __builtin_amdgcn_permlane32_swap(asu(t[0]), asu(t[2]), false, false);
      const float ra = asf(r0[0]) + asf(r0[1]);
      COMB(3);
      auto r1 = __builtin_amdgcn_permlane32_swap(asu(t[1]), asu(t[3]), false, false);
      auto r2 = __builtin_amdgcn_permlane16_swap(asu(ra), asu(asf(r1[0]) + asf(r1[1])), false, false);
      *(LAS float*)(L + red_w) = asf(r2[0]) + asf(r2[1]);
      BAR();
      const LAS f32x2* rp = (const LAS f32x2*)(L + red_r);
      const f32x2 s = ((rp[0] + rp[32]) + xr) + (rp[64] + rp[96]);
      st32(nosent(pkbf(s[0], s[1])), rs, p3_off);
      STAMP(8);
    } else {
      Q_ISSUE();
      Q_FINISH();
    }
    if (is_f) {
      const int c2 = w * 128 + m * 8, ci = kg * 4 + (m & 3);
      v4u W2r[4];
      u32 b1r;
#define W2_LOAD() { \
        b1r = ldp<u16>(b1 + f0 + ci); \
        const u16* p2 = w2 + (size_t)(f0 + kg * 4) * D + (c2 < D ? c2 : 0); \
        _Pragma("unroll") for (int i = 0; i < 4; ++i) W2r[i] = ldx<v4u, NT_TAIL>(p2 + i * D); \
        SCHED(); }
      W2_LOAD();
      if (TAIL_GATE & 2) wait_flag(L, 7, 4);
      for (int i = 0; i < FDELAY; ++i) __builtin_amdgcn_s_sleep(4);
      SCHED();
      const int c = tid & 1, rq = tid >> 1, rqc = rq < 112 ? rq : 111;
      v4u W1r[4];
      {
        const u16* p = w1 + (size_t)(rqc * 4) * F + f0 + c * 8;
#pragma unroll
        for (int i = 0; i < 4; ++i) W1r[i] = ldx<v4u, NT_WQ>(p + ((i + ROT * f) & 3) * F);
        SCHED();
      }
      v2u B2[8];
#pragma unroll
      for (int jj = 0; jj < 8; ++jj) {
        B2[jj].x = (jj & 1) ? phi(W2r[1][jj >> 1], W2r[0][jj >> 1]) : plo(W2r[1][jj >> 1], W2r[0][jj >> 1]);
        B2[jj].y = (jj & 1) ? phi(W2r[3][jj >> 1], W2r[2][jj >> 1]) : plo(W2r[3][jj >> 1], W2r[2][jj >> 1]);
        asm volatile("" : "+v"(B2[jj]));
      }
      float b1v = bflo(b1r);
#pragma unroll
      for (int i = 0; i < 4; ++i) *(LAS v4u*)(L + L_W1 + (rq * 4 + ((i + ROT * f) & 3)) * 32 + c * 16) = rq < 112 ? W1r[i] : v4u{0u, 0u, 0u, 0u};
      LDSW();
      v4u Bf[4];
#pragma unroll
      for (int s = 0; s < 4; ++s) {
        LAS unsigned char* tp = L + L_W1 + ((4 * w + s) * 32 + kg * 8 + (m >> 2)) * 32 + (m & 3) * 8;
        const v2u lo = __builtin_bit_cast(v2u, __builtin_amdgcn_ds_read_tr16_b64_v4i16((LAS s16x4*)tp));
        const v2u hi = __builtin_bit_cast(v2u, __builtin_amdgcn_ds_read_tr16_b64_v4i16((LAS s16x4*)(tp + 128)));
        Bf[s] = v4u{lo.x, lo.y, hi.x, hi.y};
      }
      const v4u ones = {0x3F803F80u, 0x3F803F80u, 0x3F803F80u, 0x3F803F80u};
      f32x4 cs = z;
#pragma unroll
      for (int s = 0; s < 4; ++s) cs = mfma(ones, Bf[s], cs);
      float csv = cs[0] * 0.25f;
      asm volatile("" : "+v"(csv), "+v"(b1v));
      STAMP(4);
      __builtin_amdgcn_s_setprio(2);
      wait_flag(L, 4, 1);
      STAMP(5);
      const int a_off = P3_OFF + rp3 * P3R + m * P3S + w * 256 + kg * 16;
      const int a_off2 = w < 3 ? a_off + 128 : OOB;
      int red_w = L_RED + w * 256 + m * 16 + kg * 4, red_r = L_RED + ci * 16;
      int p4_off = (kg < 2 && c2 < D) ? P4_OFF + f * P4S + w * 512 + kg * 256 + m * 16 : OOB;
      v4u onev = ones;
      asm volatile("" : "+v"(red_w), "+v"(red_r), "+v"(p4_off), "+v"(onev));
      v4u A[4] = {sent4, sent4, sent4, sent4};
      bool ba = true, bb = true;
      f32x4 U, G, S;
      u32 spins = 0;
      do {
        SPIN_TOP();
        if (!SELP || ba) { A[0] = ld128(rs, a_off); A[1] = ld128(rs, a_off + 64); }
        if (!SELP || bb) { A[2] = ld128(rs, a_off2); A[3] = ld128(rs, a_off2 + 64); }
        SCHED();
        G = mfma(A[0], A[0], z); SCHED();
        const u32 ma = umax4(A[0]); SCHED();
        G = mfma(A[1], A[1], G); SCHED();
        ba = umax2(ma, umax4(A[1])) > thr; SCHED();
        G = mfma(A[2], A[2], G); SCHED();
        const u32 mb = umax4(A[2]); SCHED();
        G = mfma(A[3], A[3], G); SCHED();
        bb = umax2(mb, umax4(A[3])) > thr; SCHED();
        U = mfma(A[0], Bf[0], z); S = mfma(A[0], onev, z); SCHED();
      } while (ANY2(ba, bb));
      STAMP(7);
      U = mfma(A[1], Bf[1], U); SCHED();
      float gq = (G[0] + G[1]) + (G[2] + G[3]); SCHED();
      S = mfma(A[1], onev, S); SCHED();
      gq += dpp<DPP_XOR1>(gq); gq += dpp<DPP_XOR2>(gq); SCHED();
      U = mfma(A[2], Bf[2], U); SCHED();
      gq += dpp<DPP_ROR4>(gq); gq += dpp<DPP_ROR8>(gq); SCHED();
      S = mfma(A[2], onev, S); SCHED();
      auto r1 = __builtin_amdgcn_permlane32_swap(asu(csv), asu(gq), false, false); SCHED();
      U = mfma(A[3], Bf[3], U); SCHED();
      const float rb = asf(r1[0]) + asf(r1[1]); SCHED();
      S = mfma(A[3], onev, S); SCHED();
      const f32x2 u2 = U.xy + U.zw, s2 = S.xy + S.zw;
      auto r0 = __builtin_amdgcn_permlane32_swap(asu(u2.x + u2.y), asu(s2.x + s2.y), false, false);
      auto r2 = __builtin_amdgcn_permlane16_swap(asu(asf(r0[0]) + asf(r0[1])), asu(rb), false, false);
      *(LAS float*)(L + red_w) = asf(r2[0]) + asf(r2[1]);
      BAR();
      const LAS f32x4* rp = (const LAS f32x4*)(L + red_r);
      const f32x4 t = (rp[0] + rp[16]) + (rp[32] + rp[48]);
      const float mu2 = t[2] * (1.0f / D);
      const float var = fmaxf(t[3] * (1.0f / D) - mu2 * mu2, 0.f);
      const float rstd2 = __builtin_amdgcn_rsqf(var + 1e-5f);
      const float u = (t[0] - mu2 * t[1]) * rstd2 + b1v;
      const float n = __builtin_amdgcn_exp2f(-fabsf(u) * 1.4426950408889634f);
      const float num = u > 0.f ? 1.f + 2.f * n : n * n + 2.f * n;
      const float den = u > 0.f ? num + 2.f * n * n : num + 2.f;
      const float act = u * num * __builtin_amdgcn_rcpf(den);
      const u32 ap = pkbf(act, dpp<DPP_XOR1>(act));
      v2u a2; a2.x = asu(dpp<0x00>(asf(ap))); a2.y = asu(dpp<0xAA>(asf(ap)));
      f32x4 o2[8];
#pragma unroll
      for (int jj = 0; jj < 8; ++jj)
        o2[jj] = __builtin_amdgcn_mfma_f32_16x16x16bf16_1k(__builtin_bit_cast(s16x4, a2), __builtin_bit_cast(s16x4, B2[jj]), z, 0, 0, 0);
      v4u ov;
#pragma unroll
      for (int i = 0; i < 4; ++i) ov[i] = nosent(asu((kg & 1) ? o2[4 + i][0] : o2[i][0]));
      st128(ov, rs, p4_off);
      STAMP(8);
    }
    FIN();
  }
}

extern "C" void kernel_launch(void* const* d_in, const int* in_sizes, int n_in, void* d_out, int out_size, void* d_ws,
                              size_t ws_size, hipStream_t stream) {
  fused_block<<<NWG, NTHR, 0, stream>>>((const u16*)d_in[0], (const u16*)d_in[1], (const u16*)d_in[2], (const u16*)d_in[3],
                                        (const u16*)d_in[4], (const u16*)d_in[6], (const u16*)d_in[7], (const u16*)d_in[8],
                                        (u16*)d_out, (unsigned char*)d_ws);
}
```

```cpp
#include <hip/hip_runtime.h>
#include <stdint.h>

#define GAS __attribute__((address_space(1)))
#define LAS __attribute__((address_space(3)))
typedef unsigned u32;
typedef unsigned short u16;
typedef u32 v4u __attribute__((ext_vector_type(4)));
typedef u32 v2u __attribute__((ext_vector_type(2)));
typedef float f32x4 __attribute__((ext_vector_type(4)));
typedef float f32x2 __attribute__((ext_vector_type(2)));
typedef short bf16x8 __attribute__((ext_vector_type(8)));
typedef short s16x4 __attribute__((ext_vector_type(4)));
typedef __bf16 bf16x2 __attribute__((ext_vector_type(2)));

#define NT_LOADS 1
#define NT_WQ 0
#define NT_TAIL 1
#define NK0 8
#ifndef NV0
#define NV0 2
#endif
#define TAIL_GATE 2
#define PRETOUCH 0
#define QDELAY 3
#define QSLEEP 0
#define WO_EARLY 0
#define ODELAY 0
#define KDELAY 2
#define CODEWARM 1
#define CODE_BYTES 11008
#define PDELAY 18
#define NKW 2
#define VPRE 0
#define FSTAMP 0
#define W2_EARLY 1
#define ROT 1
#define MAPB 1
#define GSH 1
#define RDELAY 0
#define FDELAY 0
#define SELP 1
#define PREAD 3
#define STAMPS 0

constexpr int D = 448, DH = 256, SK = 8192, WIN = 2048, F = 1792, NQ = 4096;
constexpr int NWG = 256, NTHR = 576;
constexpr int AUX_SC1 = 16;
constexpr u32 SENT = 0xFFFFFFFFu;
constexpr u32 SPIN_CAP = 1u << 18;
constexpr int EPOCH_OFF = 0;
constexpr int RDY_OFF = 1024;
constexpr int Q1_OFF = 4096;
constexpr int S2_OFF = Q1_OFF + 65536;
constexpr int P2S = 1152, P3S = 896, P3R = 16 * P3S + 128, P4S = 1920;
constexpr int P2_OFF = 81920;
constexpr int P3_OFF = P2_OFF + 256 * P2S;
constexpr int P4_OFF = P3_OFF + 8 * P3R;
constexpr int WS_BYTES = P4_OFF + 112 * P4S;
constexpr int STAMP_OFF = 2 << 20;

__device__ __forceinline__ float asf(u32 u) { return __builtin_bit_cast(float, u); }
__device__ __forceinline__ u32 asu(float f) { return __builtin_bit_cast(u32, f); }
__device__ __forceinline__ float bflo(u32 w) { return asf(w << 16); }
__device__ __forceinline__ float bfhi(u32 w) { return asf(w & 0xFFFF0000u); }
__device__ __forceinline__ u32 pkbf(float lo, float hi) {
  bf16x2 r; r.x = (__bf16)lo; r.y = (__bf16)hi; return __builtin_bit_cast(u32, r);
}
__device__ __forceinline__ u32 nosent(u32 v) { return v < 0xFFFFFFFEu ? v : 0xFFFFFFFEu; }
template <int CTRL> __device__ __forceinline__ float dpp(float x) {
  return asf(__builtin_amdgcn_update_dpp(0, asu(x), CTRL, 0xF, 0xF, true));
}
#define DPP_XOR1 0xB1
#define DPP_XOR2 0x4E
#define DPP_ROR4 0x124
#define DPP_ROR8 0x128
__device__ __forceinline__ float add_x16(float x) {
  auto r = __builtin_amdgcn_permlane16_swap(asu(x), asu(x), false, false);
  return asf(r[0]) + asf(r[1]);
}
__device__ __forceinline__ float add_x32(float x) {
  auto r = __builtin_amdgcn_permlane32_swap(asu(x), asu(x), false, false);
  return asf(r[0]) + asf(r[1]);
}
__device__ __forceinline__ float max_x16(float x) {
  auto r = __builtin_amdgcn_permlane16_swap(asu(x), asu(x), false, false);
  return fmaxf(asf(r[0]), asf(r[1]));
}
__device__ __forceinline__ float max_x32(float x) {
  auto r = __builtin_amdgcn_permlane32_swap(asu(x), asu(x), false, false);
  return fmaxf(asf(r[0]), asf(r[1]));
}
__device__ __forceinline__ float row_sum16(float x) {
  x += dpp<DPP_XOR1>(x); x += dpp<DPP_XOR2>(x); x += dpp<DPP_ROR4>(x); x += dpp<DPP_ROR8>(x); return x;
}
__device__ __forceinline__ float wave_sum(float x) { return add_x32(add_x16(row_sum16(x))); }
__device__ __forceinline__ f32x4 mfma(v4u a, v4u b, f32x4 c) {
  return __builtin_amdgcn_mfma_f32_16x16x32_bf16(__builtin_bit_cast(bf16x8, a), __builtin_bit_cast(bf16x8, b), c, 0, 0, 0);
}
__device__ __forceinline__ float dot2(u32 a, u32 b, float c) {
  return __builtin_amdgcn_fdot2_f32_bf16(__builtin_bit_cast(bf16x2, a), __builtin_bit_cast(bf16x2, b), c, false);
}
__device__ __forceinline__ u32 plo(u32 hi, u32 lo) { return __builtin_amdgcn_perm(hi, lo, 0x05040100u); }
__device__ __forceinline__ u32 phi(u32 hi, u32 lo) { return __builtin_amdgcn_perm(hi, lo, 0x07060302u); }
template <typename T> __device__ __forceinline__ T ldg(const void* p) {
  return __builtin_nontemporal_load((const GAS T*)p);
}
template <typename T, int NT> __device__ __forceinline__ T ldx(const void* p) {
  if (NT) return __builtin_nontemporal_load((const GAS T*)p); else return *(const GAS T*)p;
}
template <typename T> __device__ __forceinline__ T ldp(const void* p) { return *(const GAS T*)p; }
#define BAR() do { __builtin_amdgcn_sched_barrier(0); asm volatile("s_waitcnt lgkmcnt(0)\n\ts_barrier" ::: "memory"); __builtin_amdgcn_sched_barrier(0); } while (0)
#define CFENCE() asm volatile("" ::: "memory")
#define SCHED() __builtin_amdgcn_sched_barrier(0)
__device__ __forceinline__ v4u ld128(__amdgpu_buffer_rsrc_t rs, int off) { return __builtin_amdgcn_raw_buffer_load_b128(rs, off, 0, AUX_SC1); }
__device__ __forceinline__ v2u ld64(__amdgpu_buffer_rsrc_t rs, int off) { return __builtin_amdgcn_raw_buffer_load_b64(rs, off, 0, AUX_SC1); }
__device__ __forceinline__ u32 ld32(__amdgpu_buffer_rsrc_t rs, int off) { return __builtin_amdgcn_raw_buffer_load_b32(rs, off, 0, AUX_SC1); }
__device__ __forceinline__ void st128(v4u v, __amdgpu_buffer_rsrc_t rs, int off) { __builtin_amdgcn_raw_buffer_store_b128(v, rs, off, 0, AUX_SC1); }
__device__ __forceinline__ void st64(v2u v, __amdgpu_buffer_rsrc_t rs, int off) { __builtin_amdgcn_raw_buffer_store_b64(v, rs, off, 0, AUX_SC1); }
__device__ __forceinline__ void st32(u32 v, __amdgpu_buffer_rsrc_t rs, int off) { __builtin_amdgcn_raw_buffer_store_b32(v, rs, off, 0, AUX_SC1); }
__device__ __forceinline__ u32 umax4(v4u v) { u32 a = v.x > v.y ? v.x : v.y, b = v.z > v.w ? v.z : v.w; return a > b ? a : b; }
__device__ __forceinline__ u32 umax2(u32 a, u32 b) { return a > b ? a : b; }

#define STAMP(i) SCHED()
#define STAMP_OUT()

constexpr int L_QP = 0;
constexpr int L_WT = 1024;
constexpr int L_SC = 2048;
constexpr int L_FL = 2560;
constexpr int L_RED = 2816;
constexpr int L_QS = 4096;
constexpr int L_W1 = 8448;
constexpr int L_BYTES = L_W1 + 16384;
constexpr int OOB = 0x7FFFFF00;
#define LDSW() asm volatile("s_waitcnt lgkmcnt(0)" ::: "memory")
#define SPIN_END(cond) if (cond) break; if (++spins > SPIN_CAP) break
#define SPIN_TOP() u32 thr = spins++ > SPIN_CAP ? SENT : SENT - 1; asm volatile("" : "+s"(thr) :: "memory")
#define ANY2(a, b) ((__builtin_amdgcn_ballot_w64(a) | __builtin_amdgcn_ballot_w64(b)) != 0)

__device__ __forceinline__ void wait_flag(LAS unsigned char* L, int idx, u32 n) {
  for (u32 spins = 0;;) {
    CFENCE();
    const u32 fl = *(volatile LAS u32*)(L + L_FL + idx * 4);
    SPIN_END(__builtin_amdgcn_readfirstlane(fl) >= n);
    __builtin_amdgcn_s_sleep(1);
  }
}
__device__ __forceinline__ void bump_flag(LAS unsigned char* L, int idx, int lane) {
  if (lane == 0) __hip_atomic_fetch_add((LAS u32*)(L + L_FL + idx * 4), 1u, __ATOMIC_RELAXED, __HIP_MEMORY_SCOPE_WORKGROUP);
}

typedef u32 v16u __attribute__((ext_vector_type(16)));
__device__ __forceinline__ const u16* mkptr(u32 lo, u32 hi) { return (const u16*)(((unsigned long long)hi << 32) | lo); }
#define ARGS() \
  v16u ka; v4u kb; \
  asm volatile("s_load_dwordx16 %0, %2, 0x0\n\ts_load_dwordx4 %1, %2, 0x40\n\ts_waitcnt lgkmcnt(0)" : "=&s"(ka), "=&s"(kb) : "s"(__builtin_amdgcn_kernarg_segment_ptr()), "s"(kp0), "s"(kp1)); \
  const u16 *x = mkptr(ka[0], ka[1]), *Kc = mkptr(ka[2], ka[3]), *Vc = mkptr(ka[4], ka[5]), *wq = mkptr(ka[6], ka[7]); \
  const u16 *wo = mkptr(ka[8], ka[9]), *w1 = mkptr(ka[10], ka[11]), *b1 = mkptr(ka[12], ka[13]), *w2 = mkptr(ka[14], ka[15]); \
  u16* out = (u16*)mkptr(kb[0], kb[1]); \
  unsigned char* ws = (unsigned char*)mkptr(kb[2], kb[3]); \
  const __amdgpu_buffer_rsrc_t rs = __builtin_amdgcn_make_buffer_rsrc((void*)ws, 0, WS_BYTES, 0x00020000); \
  (void)x; (void)Kc; (void)Vc; (void)wq; (void)wo; (void)w1; (void)b1; (void)w2; (void)out
#define FIN() STAMP(10); STAMP_OUT(); __builtin_amdgcn_endpgm()

__global__ void __launch_bounds__(NTHR, 3)
fused_block(const u16*, const u16*, const u16*, const u16*, const u16*, const u16*, const u16*, const u16*, u16*, unsigned char*) {
  u32 kp0, kp1;
  asm volatile("s_load_dword %0, %2, 0x0\n\ts_load_dword %1, %2, 0x40" : "=&s"(kp0), "=&s"(kp1) : "s"(__builtin_amdgcn_kernarg_segment_ptr()));
  SCHED();
  unsigned long long pc0;
  asm volatile("s_getpc_b64 %0" : "=s"(pc0));
  __shared__ __attribute__((aligned(16))) unsigned char smem[L_BYTES];
  LAS unsigned char* L = (LAS unsigned char*)smem;
  const int tid = threadIdx.x, lane = tid & 63, w = __builtin_amdgcn_readfirstlane(tid >> 6);
  const int b = blockIdx.x, g = b & 7, j = b >> 3;
  const bool is_o = j < 28 && g < 4, is_f = j < 28 && g >= 4;
  const int jo = j >= 14 ? j - 14 : j, gs = j >= 14 ? g + 4 : (g + 1) & 3;
  const int f = (g - 4) * 28 + j, rp3 = (g - 4) * 2 + (j & 1);
  const int h = 2 * gs + (jo >= 7 ? 1 : 0), nc = jo >= 7 ? jo - 7 : jo;
  const int f0 = f * 16;
  const int m = lane & 15, kg = lane >> 4;
  const f32x4 z = {0.f, 0.f, 0.f, 0.f};
  const v4u sent4 = {SENT, SENT, SENT, SENT};
  STAMP(0);
  if (__builtin_expect(w >= 8, 0)) {
    int c0 = P2_OFF + (g * 32 + j) * P2S + lane * 16, c1 = lane < 8 ? P2_OFF + (g * 32 + j) * P2S + 1024 + lane * 16 : OOB;
    int c2_ = is_o ? P3_OFF + (lane >> 3) * P3R + h * P3S + nc * 128 + (lane & 7) * 16 : is_f ? P4_OFF + f * P4S + lane * 16 : OOB;
    int c3 = (is_f && lane < 56) ? P4_OFF + f * P4S + 1024 + lane * 16 : OOB;
    v4u sv4 = sent4;
    asm volatile("" : "+v"(c0), "+v"(c1), "+v"(c2_), "+v"(c3), "+v"(sv4));
    SCHED();
    ARGS();
    const u32 ep = ld32(rs, EPOCH_OFF);
#define PT_ALL() \
    u32 pt = 0; \
    if (lane < 8) { \
      const int hr = is_o ? h * 256 : 0, fr = is_f ? f0 : 0; \
      const u16* pa = (lane & 1) ? (const u16*)out : Vc + ((size_t)g * SK + (SK - WIN) + j * 64) * DH; \
      const u16* pb = wo + (size_t)(hr + (lane & 1) * 255) * D; \
      const u16* pc = w1 + (size_t)((lane & 1) * 447) * F + fr; \
      const u16* pd = (lane & 1) ? b1 + fr : w2 + (size_t)fr * D; \
      pt = ldp<u32>((lane & 4) ? ((lane & 2) ? pd : pc) : ((lane & 2) ? pb : pa)); \
    } \
    SCHED()
    st128(sv4, rs, c0); st128(sv4, rs, c1); st128(sv4, rs, c2_); st128(sv4, rs, c3);
    SCHED();
    BAR();
    asm volatile("s_waitcnt vmcnt(0)" ::: "memory");
    const u32 tag = __builtin_amdgcn_readfirstlane(ep) + 1u;
    st32(tag, rs, lane == 0 ? RDY_OFF + (g * 32 + j) * 4 : OOB);
    SCHED();
    STAMP(1);
    if (j >= 28) {
      const unsigned char* cp = (const unsigned char*)(pc0 & ~255ull) + (j - 28) * (CODE_BYTES / 4) + (lane < CODE_BYTES / 256 ? lane : 0) * 64;
      const u32 cv = ldp<u32>(cp);
      asm volatile("" :: "v"(cv));
    }
    if (is_o || is_f) {
      wait_flag(L, is_o ? 6 : 3, 1);
      for (int i = 0; i < RDELAY; ++i) __builtin_amdgcn_s_sleep(4);
      const int rdy_off = RDY_OFF + (is_o ? gs * 128 + (lane & 7) * 16 : lane * 16);
      for (u32 spins = 0;;) {
        CFENCE();
        const v4u r = ld128(rs, rdy_off);
        SPIN_END(__builtin_amdgcn_ballot_w64(!(r.x == tag && r.y == tag && r.z == tag && r.w == tag)) == 0);
        __builtin_amdgcn_s_sleep(8);
      }
      CFENCE();
      *(volatile LAS u32*)(L + L_FL + 16) = 1u;
      STAMP(5);
    }
    if (is_o) {
      __builtin_amdgcn_s_setprio(2);
      wait_flag(L, 7, 4);
      for (int i = 0; i < ODELAY; ++i) __builtin_amdgcn_s_sleep(1);
      const int s_off = P2_OFF + (gs * 32 + (lane & 31)) * P2S + 1024 + (h & 1) * 64;
      u32 sv = SENT;
      for (u32 spins = 0;;) {
        CFENCE();
        if (sv == SENT && lane < 32) sv = ld32(rs, s_off);
        SPIN_END((u32)__builtin_amdgcn_ballot_w64(sv == SENT) == 0);
      }
      STAMP(6);
      float M = asf(sv);
      M = fmaxf(M, dpp<DPP_XOR1>(M)); M = fmaxf(M, dpp<DPP_XOR2>(M)); M = fmaxf(M, dpp<DPP_ROR4>(M)); M = fmaxf(M, dpp<DPP_ROR8>(M));
      M = max_x16(M);
      const float e = __builtin_amdgcn_exp2f(asf(sv) - M);
      const float Ls = add_x16(row_sum16(e));
      if (lane < 32) *(volatile LAS u32*)(L + L_WT + lane * 4) = nosent(asu(e * __builtin_amdgcn_rcpf(Ls)));
      STAMP(7);
    }
    if (is_f) {
      __builtin_amdgcn_s_setprio(2);
      const int fo = (f >> 5) * 512 + (f & 1) * 256 + ((f >> 1) & 15) * 16;
      const int o0 = P4_OFF + lane * P4S + fo, o1 = lane < 48 ? P4_OFF + (64 + lane) * P4S + fo : OOB;
      BAR();
      const v2u hp = ld64(rs, P3_OFF + rp3 * P3R + m * P3S + f * 8);
      if (PDELAY) __builtin_amdgcn_s_sleep(PDELAY);
      v4u r0 = sent4, r1 = {hp.x << 16, hp.x & 0xFFFF0000u, hp.y << 16, hp.y & 0xFFFF0000u};
      GAS u16* op = (GAS u16*)out + f * 4 + kg;
      asm volatile("" : "+v"(op), "+v"(r1));
      bool ba = true, bb = lane < 48;
      u32 spins = 0;
      float ta, tb;
      do {
        SPIN_TOP();
        if (ba) r0 = ld128(rs, o0);
        if (bb) r1 = ld128(rs, o1);
        SCHED();
        ba = umax4(r0) > thr; bb = umax4(r1) > thr;
        SCHED();
        const f32x4 v = __builtin_bit_cast(f32x4, r0) + __builtin_bit_cast(f32x4, r1);
        auto t0 = __builtin_amdgcn_permlane32_swap(asu(v[0]), asu(v[2]), false, false);
        auto t1 = __builtin_amdgcn_permlane32_swap(asu(v[1]), asu(v[3]), false, false);
        ta = asf(t0[0]) + asf(t0[1]); tb = asf(t1[0]) + asf(t1[1]);
      } while (ANY2(ba, bb));
      STAMP(8);
      auto t2 = __builtin_amdgcn_permlane16_swap(asu(ta), asu(tb), false, false);
      const float u0 = row_sum16(asf(t2[0]) + asf(t2[1]));
      if (m == 0) {
        *op = (u16)pkbf(u0, u0);
        if (f == 0 && lane == 0) st32(tag, rs, EPOCH_OFF);
      }
      STAMP(9);
    }
    FIN();
  }
  if (__builtin_expect(w >= 4, 0)) {
    const int a = w - 4;
    if (a < 3) *(LAS v4u*)(L + (tid - 256) * 16) = tid < 416 ? sent4 : v4u{0u, 0u, 0u, 0u};
    u32 ko = ((g * SK + (SK - WIN) + j * 64 + a * 16 + m) * DH + kg * 8) * 2;
    u32 vo = ((g * SK + (SK - WIN) + j * 64 + (lane >> 3) * 8) * DH + a * 64 + (lane & 7) * 8) * 2;
    asm volatile("" : "+v"(ko), "+v"(vo));
    SCHED();
    ARGS();
    const u16* Kp = (const u16*)((const unsigned char*)Kc + ko);
    const u16* Vp = (const u16*)((const unsigned char*)Vc + vo);
    BAR();
    __builtin_amdgcn_s_setprio(1);
    v4u Kr[8], Vr[8];
#pragma unroll
    for (int i = 0; i < VPRE; ++i) Vr[i] = ldg<v4u>(Vp + i * DH);
    SCHED();
    if (a >= NKW) wait_flag(L, 5, 1);
    SCHED();
#pragma unroll
    for (int ks = 0; ks < 8; ++ks) Kr[ks] = ldg<v4u>(Kp + ks * 32);
    SCHED();
    if (a < NKW) wait_flag(L, 5, 1);
    SCHED();
#pragma unroll
    for (int i = VPRE; i < NV0; ++i) Vr[i] = ldg<v4u>(Vp + i * DH);
    SCHED();
    if (NV0 < 8) {
      wait_flag(L, 6, 4);
      SCHED();
#pragma unroll
      for (int i = NV0; i < 8; ++i) Vr[i] = ldg<v4u>(Vp + i * DH);
      SCHED();
    }
    bump_flag(L, 8, lane);
    STAMP(1);
    int qa_a = L_QS + (m & 7) * 528 + kg * 16, p_w = L_SC + (kg & 1) * 128 + (a * 16 + m) * 2, st_w = L_SC + 256 + (kg & 1) * 32 + a * 8;
    int pa_a = L_SC + (m >> 3) * 128 + (kg * 2 + ((m >> 2) & 1)) * 16, st_r = L_SC + 256 + (kg >> 1) * 32;
    int o_off = !(lane & 8) ? P2_OFF + (g * 32 + j) * P2S + (kg >> 1) * 512 + a * 128 + (kg & 1) * 64 + (lane & 7) * 8 : OOB;
    int l_off = (a == 0 && !(kg & 1)) ? P2_OFF + (g * 32 + j) * P2S + 1024 + (kg >> 1) * 64 + m * 4 : OOB;
    u32 keep = (m & 3) == kg ? 0xFFFFFFFFu : 0u;
    asm volatile("" : "+v"(qa_a), "+v"(p_w), "+v"(st_w), "+v"(pa_a), "+v"(st_r), "+v"(o_off), "+v"(l_off), "+v"(keep));
    for (u32 spins = 0;;) {
      CFENCE();
      const v4u fl4 = *(volatile LAS v4u*)(L + L_FL);
      SPIN_END(__builtin_amdgcn_readfirstlane(fl4.x & fl4.y & fl4.z & fl4.w) != 0);
    }
    CFENCE();
    v4u qa[8];
#pragma unroll
    for (int ks = 0; ks < 8; ++ks) qa[ks] = *(LAS v4u*)(L + qa_a + ks * 64);
    STAMP(2);
    f32x4 acc = z;
#pragma unroll
    for (int ks = 0; ks < 8; ++ks) acc = mfma(qa[ks], Kr[ks], acc);
    {
      const f32x2 a2 = acc.xy + acc.zw;
      float sv = a2.x + a2.y;
      sv = bflo(pkbf(sv, sv)) * (0.0625f * 1.4426950408889634f);
      float mw;
      asm volatile("s_nop 1\n\tv_max_f32_dpp %0, %1, %1 quad_perm:[1,0,3,2] row_mask:0xf bank_mask:0xf\n\t"
                   "s_nop 1\n\tv_max_f32_dpp %0, %0, %0 quad_perm:[2,3,0,1] row_mask:0xf bank_mask:0xf\n\t"
                   "s_nop 1\n\tv_max_f32_dpp %0, %0, %0 row_ror:4 row_mask:0xf bank_mask:0xf\n\t"
                   "s_nop 1\n\tv_max_f32_dpp %0, %0, %0 row_ror:8 row_mask:0xf bank_mask:0xf" : "=&v"(mw) : "v"(sv));
      const float p = __builtin_amdgcn_exp2f(sv - mw);
      *(volatile LAS u16*)(L + p_w) = (u16)(pkbf(p, p) & 0x7FFFu);
      const float lw = row_sum16(p);
      if (m == 0) *(volatile LAS v2u*)(L + st_w) = v2u{nosent(asu(mw)), nosent(asu(lw))};
    }
    STAMP(3);
    v4u B[8], pr, s0, s1;
#define P_READ() CFENCE(); pr = *(LAS v4u*)(L + pa_a); s0 = *(LAS v4u*)(L + st_r); s1 = *(LAS v4u*)(L + st_r + 16)
#pragma unroll
    for (int ii = 0; ii < 4; ++ii) {
      if (ii == PREAD) { P_READ(); SCHED(); }
#pragma unroll
      for (int jj = 0; jj < 8; ++jj)
        B[jj][ii] = (jj & 1) ? phi(Vr[2 * ii + 1][jj >> 1], Vr[2 * ii][jj >> 1]) : plo(Vr[2 * ii + 1][jj >> 1], Vr[2 * ii][jj >> 1]);
      SCHED();
    }
    asm volatile("" : "+v"(B[0]), "+v"(B[1]), "+v"(B[2]), "+v"(B[3]), "+v"(B[4]), "+v"(B[5]), "+v"(B[6]), "+v"(B[7]));
    STAMP(7);
    v4u pa;
    f32x4 c[8];
    {
      u32 spins = 0;
      bool bad;
      do {
        if (PREAD > 3 || spins) { P_READ(); }
        u32 mk = spins++ > SPIN_CAP ? 0u : 0x80008000u;
        asm volatile("" : "+s"(mk) :: "memory");
        SCHED();
        pa = pr & keep;
        c[0] = mfma(pa, B[0], z); SCHED();
        bad = (((pr.x | pr.y) | (pr.z | pr.w)) & mk) != 0; SCHED();
        c[4] = mfma(pa, B[4], z); SCHED();
      } while (__builtin_amdgcn_ballot_w64(bad) != 0);
    }
    float M, Ls;
    u32 lse;
    f32x4 fw;
#define STATS_A() M = fmaxf(fmaxf(asf(s0.x), asf(s0.z)), fmaxf(asf(s1.x), asf(s1.z))); \
    fw = f32x4{__builtin_amdgcn_exp2f(asf(s0.x) - M), __builtin_amdgcn_exp2f(asf(s0.z) - M), __builtin_amdgcn_exp2f(asf(s1.x) - M), __builtin_amdgcn_exp2f(asf(s1.z) - M)}
#define STATS_B() Ls = (fw[0] * asf(s0.y) + fw[1] * asf(s0.w)) + (fw[2] * asf(s1.y) + fw[3] * asf(s1.w)); fw *= __builtin_amdgcn_rcpf(Ls); \
    lse = nosent(asu(M + __builtin_amdgcn_logf(Ls)))
    c[1] = mfma(pa, B[1], z); SCHED();
    const bool sbad = umax2(umax4(s0), umax4(s1)) == SENT; SCHED();
    c[5] = mfma(pa, B[5], z); SCHED();
    STATS_A(); asm volatile("" : "+v"(fw), "+v"(M)); SCHED();
    c[2] = mfma(pa, B[2], z); c[6] = mfma(pa, B[6], z); SCHED();
    STATS_B(); asm volatile("" : "+v"(fw), "+v"(lse)); SCHED();
    c[3] = mfma(pa, B[3], z); c[7] = mfma(pa, B[7], z); SCHED();
    if (__builtin_expect(__builtin_amdgcn_ballot_w64(sbad) != 0, 0)) {
      for (u32 spins = 0;;) {
        CFENCE();
        s0 = *(LAS v4u*)(L + st_r); s1 = *(LAS v4u*)(L + st_r + 16);
        SPIN_END(__builtin_amdgcn_ballot_w64(umax2(umax4(s0), umax4(s1)) == SENT) == 0);
      }
      STATS_A(); STATS_B();
    }
    st32(lse, rs, l_off);
    STAMP(4);
    float os[4];
#pragma unroll
    for (int jj = 0; jj < 4; ++jj) {
      const f32x2 oa = __builtin_elementwise_fma(fw.zw, c[jj].zw, fw.xy * c[jj].xy), ob = __builtin_elementwise_fma(fw.zw, c[jj + 4].zw, fw.xy * c[jj + 4].xy);
      float o0 = oa.x + oa.y, o1 = ob.x + ob.y;
      asm volatile("" : "+v"(o0), "+v"(o1));
      auto r = __builtin_amdgcn_permlane16_swap(asu(o0), asu(o1), false, false);
      os[jj] = asf(r[0]) + dpp<DPP_ROR8>(asf(r[1]));
      asm volatile("" : "+v"(os[jj]));
    }
    {
      v2u ov; ov.x = nosent(pkbf(os[0], os[1])); ov.y = nosent(pkbf(os[2], os[3]));
      st64(ov, rs, o_off);
    }
    SCHED();
    bump_flag(L, 7, lane);
    STAMP(5);
    FIN();
  }
  {
    __builtin_amdgcn_s_setprio(3);
    const int part = j & 3, cb = j >> 2;
    const int qr = tid >> 3, qrow = part * 112 + (qr < 28 ? qr : 27) * 4;
    u32 xo = (lane < 56 ? lane : 55) * 16, xo2 = qrow * 2, wo_[4];
#pragma unroll
    for (int i = 0; i < 4; ++i) wo_[i] = ((qrow + i) * NQ + g * 512 + cb * 64 + (lane & 7) * 8) * 2;
    asm volatile("" : "+v"(xo), "+v"(xo2), "+v"(wo_[0]), "+v"(wo_[1]), "+v"(wo_[2]), "+v"(wo_[3]));
    SCHED();
    ARGS();
    const v4u xa = ldp<v4u>((const unsigned char*)x + xo);
    const v2u xq = ldp<v2u>((const unsigned char*)x + xo2);
    SCHED();
    v4u Wr[4];
#pragma unroll
    for (int i = 0; i < 4; ++i) Wr[i] = ldx<v4u, NT_LOADS>((const u16*)((const unsigned char*)wq + wo_[i]));
    SCHED();
    const u32 ep = ld32(rs, w == 0 ? EPOCH_OFF : OOB);
    SCHED();
    BAR();
    int qp_w = L_QP + w * 256 + (lane & 7) * 32 + kg * 8, qp_r = L_QP + (lane & 31) * 8;
    int pub_off = lane < 32 ? Q1_OFF + g * 8192 + part * 2048 + cb * 256 + lane * 8 : OOB;
    int q_off = Q1_OFF + g * 8192 + w * 2048 + lane * 32, qs_w = L_QS + ((lane >> 5) * 4 + w) * 528 + (lane & 31) * 16;
    float a[8] = {0.f, 0.f, 0.f, 0.f, 0.f, 0.f, 0.f, 0.f};
    asm volatile("" : "+v"(qp_w), "+v"(qp_r), "+v"(pub_off), "+v"(q_off), "+v"(qs_w), "+v"(a[0]), "+v"(a[1]), "+v"(a[2]), "+v"(a[3]), "+v"(a[4]), "+v"(a[5]), "+v"(a[6]), "+v"(a[7]));
    SCHED();
    float s1 = ((bflo(xa[0]) + bfhi(xa[0])) + (bflo(xa[1]) + bfhi(xa[1]))) + ((bflo(xa[2]) + bfhi(xa[2])) + (bflo(xa[3]) + bfhi(xa[3])));
    s1 = lane < 56 ? s1 : 0.f;
    const float mu = wave_sum(s1) * (1.0f / D);
    float s2 = 0.f, s3 = 0.f;
#pragma unroll
    for (int i = 0; i < 4; ++i) { const float aa = bflo(xa[i]) - mu, bb = bfhi(xa[i]) - mu; s2 += aa * aa; s3 += bb * bb; }
    s2 = lane < 56 ? s2 + s3 : 0.f;
    float rstd = __builtin_amdgcn_rsqf(wave_sum(s2) * (1.0f / D) + 1e-5f);
    rstd = qr < 28 ? rstd : 0.f;
    const u32 xn0 = pkbf((bflo(xq.x) - mu) * rstd, (bfhi(xq.x) - mu) * rstd);
    const u32 xn1 = pkbf((bflo(xq.y) - mu) * rstd, (bfhi(xq.y) - mu) * rstd);
    STAMP(11);
#pragma unroll
    for (int jj = 0; jj < 4; ++jj) {
      a[2 * jj] = dot2(plo(Wr[1][jj], Wr[0][jj]), xn0, a[2 * jj]);
      a[2 * jj + 1] = dot2(phi(Wr[1][jj], Wr[0][jj]), xn0, a[2 * jj + 1]);
    }
    SCHED();
#pragma unroll
    for (int jj = 0; jj < 4; ++jj) {
      const int k = (jj >> 1) | ((jj & 1) << 1);
      a[2 * k] = dot2(plo(Wr[3][k], Wr[2][k]), xn1, a[2 * k]);
      a[2 * k + 1] = dot2(phi(Wr[3][k], Wr[2][k]), xn1, a[2 * k + 1]);
    }
    float q2[2];
    {
      float t[4];
#pragma unroll
      for (int jj = 0; jj < 4; ++jj) {
        auto r = __builtin_amdgcn_permlane32_swap(asu(a[jj]), asu(a[jj + 4]), false, false);
        t[jj] = asf(r[0]) + asf(r[1]);
      }
#pragma unroll
      for (int jj = 0; jj < 2; ++jj) {
        auto r = __builtin_amdgcn_permlane16_swap(asu(t[jj]), asu(t[jj + 2]), false, false);
        q2[jj] = asf(r[0]) + asf(r[1]);
        asm volatile("" : "+v"(q2[jj]));
        q2[jj] += dpp<DPP_ROR8>(q2[jj]);
        asm volatile("" : "+v"(q2[jj]));
      }
    }
    *(LAS v2u*)(L + qp_w) = v2u{nosent(asu(q2[0])), nosent(asu(q2[1]))};
    SCHED();
    u32 tag;
    STAMP(1);
    if (w == 0) {
      tag = __builtin_amdgcn_readfirstlane(ep) + 1u;
      *(volatile LAS u32*)(L + L_FL + 36) = tag;
      u32 spins = 0;
      bool bad;
      v2u gr; gr.y = tag;
      do {
        SPIN_TOP();
        const v2u p0 = *(LAS v2u*)(L + qp_r), p1 = *(LAS v2u*)(L + qp_r + 256), p2 = *(LAS v2u*)(L + qp_r + 512), p3 = *(LAS v2u*)(L + qp_r + 768);
        bad = umax2(umax4(v4u{p0.x, p0.y, p1.x, p1.y}), umax4(v4u{p2.x, p2.y, p3.x, p3.y})) > thr;
        const f32x2 qs = (__builtin_bit_cast(f32x2, p0) + __builtin_bit_cast(f32x2, p1)) + (__builtin_bit_cast(f32x2, p2) + __builtin_bit_cast(f32x2, p3));
        gr.x = pkbf(qs.x, qs.y);
        asm volatile("" : "+v"(gr));
      } while (__builtin_amdgcn_ballot_w64(bad) != 0);
      st64(gr, rs, pub_off);
      SCHED();
      *(volatile LAS u32*)(L + L_FL + 20) = 1u;
      STAMP(2);
    }
    if (w != 0) {
      wait_flag(L, 5, 1);
      CFENCE();
      tag = __builtin_amdgcn_readfirstlane(*(volatile LAS u32*)(L + L_FL + 36));
    }
    for (int i = 0; i < QDELAY; ++i) __builtin_amdgcn_s_sleep(4);
#define Q_ISSUE() v4u g0 = ld128(rs, q_off), g1 = ld128(rs, q_off + 16); SCHED(); bump_flag(L, 6, lane); STAMP(9)
#define QCNT()
#define Q_FINISH() \
    { \
      u32 spins = 0; \
      bool bad = false; \
      do { \
        u32 mk;     \
        asm volatile("s_cmp_gt_u32 %1, %2\n\ts_cselect_b32 %0, 0, -1" : "=s"(mk) : "s"(spins++), "s"(SPIN_CAP) : "scc", "memory"); \
        if (bad) { g0 = ld128(rs, q_off); g1 = ld128(rs, q_off + 16); } \
        *(LAS v4u*)(L + qs_w) = v4u{g0.x, g0.z, g1.x, g1.z};     \
        bad = ((((g0.y ^ tag) | (g0.w ^ tag)) | ((g1.y ^ tag) | (g1.w ^ tag))) & mk) != 0; \
      } while (__builtin_amdgcn_ballot_w64(bad) != 0); \
    } \
    CFENCE(); \
    *(volatile LAS u32*)(L + L_FL + w * 4) = 1u; \
    __builtin_amdgcn_s_setprio(0); \
    STAMP(3)
    if (is_o) {
      Q_ISSUE();
      Q_FINISH();
      SCHED();
      u32 xv = ldp<u32>(x + nc * 64 + (lane & 31) * 2);
      SCHED();
      v2u r[16];
      const u16* p = wo + (size_t)(h * 256 + w * 64 + kg * 16) * D + nc * 64 + m * 4;
#pragma unroll
      for (int i = 0; i < 16; ++i) r[i] = ldx<v2u, NT_TAIL>(p + (((i >> 2) & 1) * 8 + (i >> 3) * 4 + (i & 3)) * D);
      SCHED();
      wait_flag(L, 4, 1);
      wait_flag(L, 7, 4);
      for (int i = 0; i < ODELAY; ++i) __builtin_amdgcn_s_sleep(1);
      STAMP(5);
      __builtin_amdgcn_s_setprio(2);
      const int a_off = P2_OFF + (gs * 32 + m) * P2S + (h & 1) * 512 + w * 128 + kg * 16;
      v4u A[4];
      A[0] = ld128(rs, a_off); A[1] = ld128(rs, a_off + 64); A[2] = ld128(rs, a_off + 16 * P2S); A[3] = ld128(rs, a_off + 16 * P2S + 64);
      SCHED();
      xv = h == 0 ? xv : 0u;
      v4u B[2][4];
#pragma unroll
      for (int s = 0; s < 2; ++s)
#pragma unroll
        for (int ii = 0; ii < 4; ++ii) {
          const v2u lo = r[s * 8 + 2 * ii], hi = r[s * 8 + 2 * ii + 1];
          B[s][0][ii] = plo(hi.x, lo.x); B[s][1][ii] = phi(hi.x, lo.x); B[s][2][ii] = plo(hi.y, lo.y); B[s][3][ii] = phi(hi.y, lo.y);
        }
      asm volatile("" : "+v"(xv), "+v"(B[0][0]), "+v"(B[0][1]), "+v"(B[0][2]), "+v"(B[0][3]), "+v"(B[1][0]), "+v"(B[1][1]), "+v"(B[1][2]), "+v"(B[1][3]));
      int wt_a = L_WT + kg * 16, red_w = L_RED + w * 256 + m * 16 + kg * 4, red_r = L_RED + (lane & 31) * 8;
      int p3_off = P3_OFF + (2 * w + (lane >> 5)) * P3R + h * P3S + nc * 128 + (lane & 31) * 4;
      f32x2 xr = {bflo(xv), bfhi(xv)};
      asm volatile("" : "+v"(wt_a), "+v"(red_w), "+v"(red_r), "+v"(p3_off), "+v"(xr));
      STAMP(4);
      f32x4 C0[4], C1[4];
      bool ba = false, bb = false;
      u32 spins = 0;
      do {
        SPIN_TOP();
        if (ba) { A[0] = ld128(rs, a_off); A[1] = ld128(rs, a_off + 64); }
        if (bb) { A[2] = ld128(rs, a_off + 16 * P2S); A[3] = ld128(rs, a_off + 16 * P2S + 64); }
        SCHED();
        C0[0] = mfma(A[0], B[0][0], z); SCHED();
        const u32 ma = umax4(A[0]); SCHED();
        C0[1] = mfma(A[0], B[0][1], z); SCHED();
        ba = umax2(ma, umax4(A[1])) > thr; SCHED();
        C0[0] = mfma(A[1], B[1][0], C0[0]); SCHED();
        const u32 mb = umax4(A[2]); SCHED();
        C1[0] = mfma(A[2], B[0][0], z); SCHED();
        bb = umax2(mb, umax4(A[3])) > thr; SCHED();
        C0[1] = mfma(A[1], B[1][1], C0[1]); SCHED();
        C1[0] = mfma(A[3], B[1][0], C1[0]); SCHED();
      } while (ANY2(ba, bb));
      STAMP(7);
      C1[1] = mfma(A[2], B[0][1], z); C0[2] = mfma(A[0], B[0][2], z); SCHED();
      CFENCE();
      v4u e0 = *(LAS v4u*)(L + wt_a), e1 = *(LAS v4u*)(L + wt_a + 64);
      SCHED();
      C1[1] = mfma(A[3], B[1][1], C1[1]); C1[2] = mfma(A[2], B[0][2], z); C0[2] = mfma(A[1], B[1][2], C0[2]); C0[3] = mfma(A[0], B[0][3], z); SCHED();
      C1[2] = mfma(A[3], B[1][2], C1[2]); C1[3] = mfma(A[2], B[0][3], z); SCHED();
#define COMB(jj) { \
        const f32x4 ea = __builtin_bit_cast(f32x4, e0), eb = __builtin_bit_cast(f32x4, e1); \
        f32x2 c = ea.xy * C0[jj].xy, d = eb.xy * C1[jj].xy; \
        c = __builtin_elementwise_fma(ea.zw, C0[jj].zw, c); d = __builtin_elementwise_fma(eb.zw, C1[jj].zw, d); \
        c += d; t[jj] = c.x + c.y; }
      float t[4];
      const bool be = umax2(umax4(e0), umax4(e1)) == SENT; SCHED();
      C0[3] = mfma(A[1], B[1][3], C0[3]); SCHED();
      COMB(0); asm volatile("" : "+v"(t[0])); SCHED();
      C1[3] = mfma(A[3], B[1][3], C1[3]); SCHED();
      COMB(2); COMB(1); asm volatile("" : "+v"(t[1]), "+v"(t[2])); SCHED();
      if (__builtin_expect(__builtin_amdgcn_ballot_w64(be) != 0, 0)) {
        for (u32 spins = 0;;) {
          CFENCE();
          e0 = *(LAS v4u*)(L + wt_a); e1 = *(LAS v4u*)(L + wt_a + 64);
          SPIN_END(__builtin_amdgcn_ballot_w64(umax2(umax4(e0), umax4(e1)) == SENT) == 0);
        }
        COMB(0); COMB(2); COMB(1);
      }
      auto r0 = __builtin_amdgcn_permlane32_swap(asu(t[0]), asu(t[2]), false, false);
      const float ra = asf(r0[0]) + asf(r0[1]);
      COMB(3);
      auto r1 = __builtin_amdgcn_permlane32_swap(asu(t[1]), asu(t[3]), false, false);
      auto r2 = __builtin_amdgcn_permlane16_swap(asu(ra), asu(asf(r1[0]) + asf(r1[1])), false, false);
      *(LAS float*)(L + red_w) = asf(r2[0]) + asf(r2[1]);
      BAR();
      const LAS f32x2* rp = (const LAS f32x2*)(L + red_r);
      const f32x2 s = ((rp[0] + rp[32]) + xr) + (rp[64] + rp[96]);
      st32(nosent(pkbf(s[0], s[1])), rs, p3_off);
      STAMP(8);
    } else {
      Q_ISSUE();
      Q_FINISH();
    }
    if (is_f) {
      const int c2 = w * 128 + m * 8, ci = kg * 4 + (m & 3);
      v4u W2r[4];
      u32 b1r;
#define W2_LOAD() { \
        b1r = ldp<u16>(b1 + f0 + ci); \
        const u16* p2 = w2 + (size_t)(f0 + kg * 4) * D + (c2 < D ? c2 : 0); \
        _Pragma("unroll") for (int i = 0; i < 4; ++i) W2r[i] = ldx<v4u, NT_TAIL>(p2 + i * D); \
        SCHED(); }
      W2_LOAD();
      if (TAIL_GATE & 2) wait_flag(L, 7, 4);
      for (int i = 0; i < FDELAY; ++i) __builtin_amdgcn_s_sleep(4);
      SCHED();
      const int c = tid & 1, rq = tid >> 1, rqc = rq < 112 ? rq : 111;
      v4u W1r[4];
      {
        const u16* p = w1 + (size_t)(rqc * 4) * F + f0 + c * 8;
#pragma unroll
        for (int i = 0; i < 4; ++i) W1r[i] = ldx<v4u, NT_WQ>(p + ((i + ROT * f) & 3) * F);
        SCHED();
      }
      v2u B2[8];
#pragma unroll
      for (int jj = 0; jj < 8; ++jj) {
        B2[jj].x = (jj & 1) ? phi(W2r[1][jj >> 1], W2r[0][jj >> 1]) : plo(W2r[1][jj >> 1], W2r[0][jj >> 1]);
        B2[jj].y = (jj & 1) ? phi(W2r[3][jj >> 1], W2r[2][jj >> 1]) : plo(W2r[3][jj >> 1], W2r[2][jj >> 1]);
        asm volatile("" : "+v"(B2[jj]));
      }
      float b1v = bflo(b1r);
#pragma unroll
      for (int i = 0; i < 4; ++i) *(LAS v4u*)(L + L_W1 + (rq * 4 + ((i + ROT * f) & 3)) * 32 + c * 16) = rq < 112 ? W1r[i] : v4u{0u, 0u, 0u, 0u};
      LDSW();
      v4u Bf[4];
#pragma unroll
      for (int s = 0; s < 4; ++s) {
        LAS unsigned char* tp = L + L_W1 + ((4 * w + s) * 32 + kg * 8 + (m >> 2)) * 32 + (m & 3) * 8;
        const v2u lo = __builtin_bit_cast(v2u, __builtin_amdgcn_ds_read_tr16_b64_v4i16((LAS s16x4*)tp));
        const v2u hi = __builtin_bit_cast(v2u, __builtin_amdgcn_ds_read_tr16_b64_v4i16((LAS s16x4*)(tp + 128)));
        Bf[s] = v4u{lo.x, lo.y, hi.x, hi.y};
      }
      const v4u ones = {0x3F803F80u, 0x3F803F80u, 0x3F803F80u, 0x3F803F80u};
      f32x4 cs = z;
#pragma unroll
      for (int s = 0; s < 4; ++s) cs = mfma(ones, Bf[s], cs);
      float csv = cs[0] * 0.25f;
      asm volatile("" : "+v"(csv), "+v"(b1v));
      STAMP(4);
      __builtin_amdgcn_s_setprio(2);
      wait_flag(L, 4, 1);
      STAMP(5);
      const int a_off = P3_OFF + rp3 * P3R + m * P3S + w * 256 + kg * 16;
      const int a_off2 = w < 3 ? a_off + 128 : OOB;
      int red_w = L_RED + w * 256 + m * 16 + kg * 4, red_r = L_RED + ci * 16;
      int p4_off = (kg < 2 && c2 < D) ? P4_OFF + f * P4S + w * 512 + kg * 256 + m * 16 : OOB;
      v4u onev = ones;
      asm volatile("" : "+v"(red_w), "+v"(red_r), "+v"(p4_off), "+v"(onev));
      v4u A[4] = {sent4, sent4, sent4, sent4};
      bool ba = true, bb = true;
      f32x4 U, G, S;
      u32 spins = 0;
      do {
        SPIN_TOP();
        if (!SELP || ba) { A[0] = ld128(rs, a_off); A[1] = ld128(rs, a_off + 64); }
        if (!SELP || bb) { A[2] = ld128(rs, a_off2); A[3] = ld128(rs, a_off2 + 64); }
        SCHED();
        G = mfma(A[0], A[0], z); SCHED();
        const u32 ma = umax4(A[0]); SCHED();
        G = mfma(A[1], A[1], G); SCHED();
        ba = umax2(ma, umax4(A[1])) > thr; SCHED();
        G = mfma(A[2], A[2], G); SCHED();
        const u32 mb = umax4(A[2]); SCHED();
        G = mfma(A[3], A[3], G); SCHED();
        bb = umax2(mb, umax4(A[3])) > thr; SCHED();
        U = mfma(A[0], Bf[0], z); S = mfma(A[0], onev, z); SCHED();
      } while (ANY2(ba, bb));
      STAMP(7);
      U = mfma(A[1], Bf[1], U); SCHED();
      float gq = (G[0] + G[1]) + (G[2] + G[3]); SCHED();
      S = mfma(A[1], onev, S); SCHED();
      gq += dpp<DPP_XOR1>(gq); gq += dpp<DPP_XOR2>(gq); SCHED();
      U = mfma(A[2], Bf[2], U); SCHED();
      gq += dpp<DPP_ROR4>(gq); gq += dpp<DPP_ROR8>(gq); SCHED();
      S = mfma(A[2], onev, S); SCHED();
      auto r1 = __builtin_amdgcn_permlane32_swap(asu(csv), asu(gq), false, false); SCHED();
      U = mfma(A[3], Bf[3], U); SCHED();
      const float rb = asf(r1[0]) + asf(r1[1]); SCHED();
      S = mfma(A[3], onev, S); SCHED();
      const f32x2 u2 = U.xy + U.zw, s2 = S.xy + S.zw;
      auto r0 = __builtin_amdgcn_permlane32_swap(asu(u2.x + u2.y), asu(s2.x + s2.y), false, false);
      auto r2 = __builtin_amdgcn_permlane16_swap(asu(asf(r0[0]) + asf(r0[1])), asu(rb), false, false);
      *(LAS float*)(L + red_w) = asf(r2[0]) + asf(r2[1]);
      BAR();
      const LAS f32x4* rp = (const LAS f32x4*)(L + red_r);
      const f32x4 t = (rp[0] + rp[16]) + (rp[32] + rp[48]);
      const float mu2 = t[2] * (1.0f / D);
      const float var = fmaxf(t[3] * (1.0f / D) - mu2 * mu2, 0.f);
      const float rstd2 = __builtin_amdgcn_rsqf(var + 1e-5f);
      const float u = (t[0] - mu2 * t[1]) * rstd2 + b1v;
      const float n = __builtin_amdgcn_exp2f(-fabsf(u) * 1.4426950408889634f);
      const float num = u > 0.f ? 1.f + 2.f * n : n * n + 2.f * n;
      const float den = u > 0.f ? num + 2.f * n * n : num + 2.f;
      const float act = u * num * __builtin_amdgcn_rcpf(den);
      const u32 ap = pkbf(act, dpp<DPP_XOR1>(act));
      v2u a2; a2.x = asu(dpp<0x00>(asf(ap))); a2.y = asu(dpp<0xAA>(asf(ap)));
      f32x4 o2[8];
#pragma unroll
      for (int jj = 0; jj < 8; ++jj)
        o2[jj] = __builtin_amdgcn_mfma_f32_16x16x16bf16_1k(__builtin_bit_cast(s16x4, a2), __builtin_bit_cast(s16x4, B2[jj]), z, 0, 0, 0);
      v4u ov;
#pragma unroll
      for (int i = 0; i < 4; ++i) ov[i] = nosent(asu((kg & 1) ? o2[4 + i][0] : o2[i][0]));
      st128(ov, rs, p4_off);
      STAMP(8);
    }
    FIN();
  }
}

extern "C" void kernel_launch(void* const* d_in, const int* in_sizes, int n_in, void* d_out, int out_size, void* d_ws,
                              size_t ws_size, hipStream_t stream) {
  fused_block<<<NWG, NTHR, 0, stream>>>((const u16*)d_in[0], (const u16*)d_in[1], (const u16*)d_in[2], (const u16*)d_in[3],
                                        (const u16*)d_in[4], (const u16*)d_in[6], (const u16*)d_in[7], (const u16*)d_in[8],
                                        (u16*)d_out, (unsigned char*)d_ws);
}
```

```cpp
#include <hip/hip_runtime.h>
#include <stdint.h>

#define GAS __attribute__((address_space(1)))
#define LAS __attribute__((address_space(3)))
typedef unsigned u32;
typedef unsigned short u16;
typedef u32 v4u __attribute__((ext_vector_type(4)));
typedef u32 v2u __attribute__((ext_vector_type(2)));
typedef float f32x4 __attribute__((ext_vector_type(4)));
typedef float f32x2 __attribute__((ext_vector_type(2)));
typedef short bf16x8 __attribute__((ext_vector_type(8)));
typedef short s16x4 __attribute__((ext_vector_type(4)));
typedef __bf16 bf16x2 __attribute__((ext_vector_type(2)));

#define NT_LOADS 1
#define NT_WQ 0
#define NT_TAIL 1
#define NK0 8
#ifndef NV0
#define NV0 2
#endif
#define TAIL_GATE 2
#define PRETOUCH 0
#define QDELAY 3
#define QSLEEP 0
#define WO_EARLY 0
#define ODELAY 0
#define KDELAY 2
#define CODEWARM 1
#define CODE_BYTES 11008
#define PDELAY 18
#define NKW 2
#define VPRE 0
#define FSTAMP 0
#define W2_EARLY 1
#define ROT 1
#define MAPB 1
#define GSH 1
#define RDELAY 0
#define FDELAY 0
#define SELP 1
#define PREAD 3
#define STAMPS 0

constexpr int D = 448, DH = 256, SK = 8192, WIN = 2048, F = 1792, NQ = 4096;
constexpr int NWG = 256, NTHR = 576;
constexpr int AUX_SC1 = 16;
constexpr u32 SENT = 0xFFFFFFFFu;
constexpr u32 SPIN_CAP = 1u << 18;
constexpr int EPOCH_OFF = 0;
constexpr int RDY_OFF = 1024;
constexpr int Q1_OFF = 4096;
constexpr int S2_OFF = Q1_OFF + 65536;
constexpr int P2S = 1152, P3S = 896, P3R = 16 * P3S + 128, P4S = 1920;
constexpr int P2_OFF = 81920;
constexpr int P3_OFF = P2_OFF + 256 * P2S;
constexpr int P4_OFF = P3_OFF + 8 * P3R;
constexpr int WS_BYTES = P4_OFF + 112 * P4S;
constexpr int STAMP_OFF = 2 << 20;

__device__ __forceinline__ float asf(u32 u) { return __builtin_bit_cast(float, u); }
__device__ __forceinline__ u32 asu(float f) { return __builtin_bit_cast(u32, f); }
__device__ __forceinline__ float bflo(u32 w) { return asf(w << 16); }
__device__ __forceinline__ float bfhi(u32 w) { return asf(w & 0xFFFF0000u); }
__device__ __forceinline__ u32 pkbf(float lo, float hi) {
  bf16x2 r; r.x = (__bf16)lo; r.y = (__bf16)hi; return __builtin_bit_cast(u32, r);
}
__device__ __forceinline__ u32 nosent(u32 v) { return v < 0xFFFFFFFEu ? v : 0xFFFFFFFEu; }
template <int CTRL> __device__ __forceinline__ float dpp(float x) {
  return asf(__builtin_amdgcn_update_dpp(0, asu(x), CTRL, 0xF, 0xF, true));
}
#define DPP_XOR1 0xB1
#define DPP_XOR2 0x4E
#define DPP_ROR4 0x124
#define DPP_ROR8 0x128
__device__ __forceinline__ float add_x16(float x) {
  auto r = __builtin_amdgcn_permlane16_swap(asu(x), asu(x), false, false);
  return asf(r[0]) + asf(r[1]);
}
__device__ __forceinline__ float add_x32(float x) {
  auto r = __builtin_amdgcn_permlane32_swap(asu(x), asu(x), false, false);
  return asf(r[0]) + asf(r[1]);
}
__device__ __forceinline__ float max_x16(float x) {
  auto r = __builtin_amdgcn_permlane16_swap(asu(x), asu(x), false, false);
  return fmaxf(asf(r[0]), asf(r[1]));
}
__device__ __forceinline__ float max_x32(float x) {
  auto r = __builtin_amdgcn_permlane32_swap(asu(x), asu(x), false, false);
  return fmaxf(asf(r[0]), asf(r[1]));
}
__device__ __forceinline__ float row_sum16(float x) {
  x += dpp<DPP_XOR1>(x); x += dpp<DPP_XOR2>(x); x += dpp<DPP_ROR4>(x); x += dpp<DPP_ROR8>(x); return x;
}
__device__ __forceinline__ float wave_sum(float x) { return add_x32(add_x16(row_sum16(x))); }
__device__ __forceinline__ f32x4 mfma(v4u a, v4u b, f32x4 c) {
  return __builtin_amdgcn_mfma_f32_16x16x32_bf16(__builtin_bit_cast(bf16x8, a), __builtin_bit_cast(bf16x8, b), c, 0, 0, 0);
}
__device__ __forceinline__ float dot2(u32 a, u32 b, float c) {
  return __builtin_amdgcn_fdot2_f32_bf16(__builtin_bit_cast(bf16x2, a), __builtin_bit_cast(bf16x2, b), c, false);
}
__device__ __forceinline__ u32 plo(u32 hi, u32 lo) { return __builtin_amdgcn_perm(hi, lo, 0x05040100u); }
__device__ __forceinline__ u32 phi(u32 hi, u32 lo) { return __builtin_amdgcn_perm(hi, lo, 0x07060302u); }
template <typename T> __device__ __forceinline__ T ldg(const void* p) {
  return __builtin_nontemporal_load((const GAS T*)p);
}
template <typename T, int NT> __device__ __forceinline__ T ldx(const void* p) {
  if (NT) return __builtin_nontemporal_load((const GAS T*)p); else return *(const GAS T*)p;
}
template <typename T> __device__ __forceinline__ T ldp(const void* p) { return *(const GAS T*)p; }
#define BAR() do { __builtin_amdgcn_sched_barrier(0); asm volatile("s_waitcnt lgkmcnt(0)\n\ts_barrier" ::: "memory"); __builtin_amdgcn_sched_barrier(0); } while (0)
#define CFENCE() asm volatile("" ::: "memory")
#define SCHED() __builtin_amdgcn_sched_barrier(0)
__device__ __forceinline__ v4u ld128(__amdgpu_buffer_rsrc_t rs, int off) { return __builtin_amdgcn_raw_buffer_load_b128(rs, off, 0, AUX_SC1); }
__device__ __forceinline__ v2u ld64(__amdgpu_buffer_rsrc_t rs, int off) { return __builtin_amdgcn_raw_buffer_load_b64(rs, off, 0, AUX_SC1); }
__device__ __forceinline__ u32 ld32(__amdgpu_buffer_rsrc_t rs, int off) { return __builtin_amdgcn_raw_buffer_load_b32(rs, off, 0, AUX_SC1); }
__device__ __forceinline__ void st128(v4u v, __amdgpu_buffer_rsrc_t rs, int off) { __builtin_amdgcn_raw_buffer_store_b128(v, rs, off, 0, AUX_SC1); }
__device__ __forceinline__ void st64(v2u v, __amdgpu_buffer_rsrc_t rs, int off) { __builtin_amdgcn_raw_buffer_store_b64(v, rs, off, 0, AUX_SC1); }
__device__ __forceinline__ void st32(u32 v, __amdgpu_buffer_rsrc_t rs, int off) { __builtin_amdgcn_raw_buffer_store_b32(v, rs, off, 0, AUX_SC1); }
__device__ __forceinline__ u32 umax4(v4u v) { u32 a = v.x > v.y ? v.x : v.y, b = v.z > v.w ? v.z : v.w; return a > b ? a : b; }
__device__ __forceinline__ u32 umax2(u32 a, u32 b) { return a > b ? a : b; }

#define STAMP(i) SCHED()
#define STAMP_OUT()

constexpr int L_QP = 0;
constexpr int L_WT = 1024;
constexpr int L_SC = 2048;
constexpr int L_FL = 2560;
constexpr int L_RED = 2816;
constexpr int L_QS = 4096;
constexpr int L_W1 = 8448;
constexpr int L_BYTES = L_W1 + 16384;
constexpr int OOB = 0x7FFFFF00;
#define LDSW() asm volatile("s_waitcnt lgkmcnt(0)" ::: "memory")
#define SPIN_END(cond) if (cond) break; if (++spins > SPIN_CAP) break
#define SPIN_TOP() u32 thr = spins++ > SPIN_CAP ? SENT : SENT - 1; asm volatile("" : "+s"(thr) :: "memory")
#define ANY2(a, b) ((__builtin_amdgcn_ballot_w64(a) | __builtin_amdgcn_ballot_w64(b)) != 0)

__device__ __forceinline__ void wait_flag(LAS unsigned char* L, int idx, u32 n) {
  for (u32 spins = 0;;) {
    CFENCE();
    const u32 fl = *(volatile LAS u32*)(L + L_FL + idx * 4);
    SPIN_END(__builtin_amdgcn_readfirstlane(fl) >= n);
    __builtin_amdgcn_s_sleep(1);
  }
}
__device__ __forceinline__ void bump_flag(LAS unsigned char* L, int idx, int lane) {
  if (lane == 0) __hip_atomic_fetch_add((LAS u32*)(L + L_FL + idx * 4), 1u, __ATOMIC_RELAXED, __HIP_MEMORY_SCOPE_WORKGROUP);
}

typedef u32 v16u __attribute__((ext_vector_type(16)));
__device__ __forceinline__ const u16* mkptr(u32 lo, u32 hi) { return (const u16*)(((unsigned long long)hi << 32) | lo); }
#define ARGS() \
  v16u ka; v4u kb; \
  asm volatile("s_load_dwordx16 %0, %2, 0x0\n\ts_load_dwordx4 %1, %2, 0x40\n\ts_waitcnt lgkmcnt(0)" : "=&s"(ka), "=&s"(kb) : "s"(__builtin_amdgcn_kernarg_segment_ptr()), "s"(kp0), "s"(kp1)); \
  const u16 *x = mkptr(ka[0], ka[1]), *Kc = mkptr(ka[2], ka[3]), *Vc = mkptr(ka[4], ka[5]), *wq = mkptr(ka[6], ka[7]); \
  const u16 *wo = mkptr(ka[8], ka[9]), *w1 = mkptr(ka[10], ka[11]), *b1 = mkptr(ka[12], ka[13]), *w2 = mkptr(ka[14], ka[15]); \
  u16* out = (u16*)mkptr(kb[0], kb[1]); \
  unsigned char* ws = (unsigned char*)mkptr(kb[2], kb[3]); \
  const __amdgpu_buffer_rsrc_t rs = __builtin_amdgcn_make_buffer_rsrc((void*)ws, 0, WS_BYTES, 0x00020000); \
  (void)x; (void)Kc; (void)Vc; (void)wq; (void)wo; (void)w1; (void)b1; (void)w2; (void)out
#define FIN() STAMP(10); STAMP_OUT(); __builtin_amdgcn_endpgm()

__global__ void __launch_bounds__(NTHR, 3)
fused_block(const u16*, const u16*, const u16*, const u16*, const u16*, const u16*, const u16*, const u16*, u16*, unsigned char*) {
  u32 kp0, kp1;
  asm volatile("s_load_dword %0, %2, 0x0\n\ts_load_dword %1, %2, 0x40" : "=&s"(kp0), "=&s"(kp1) : "s"(__builtin_amdgcn_kernarg_segment_ptr()));
  SCHED();
  unsigned long long pc0;
  asm volatile("s_getpc_b64 %0" : "=s"(pc0));
  __shared__ __attribute__((aligned(16))) unsigned char smem[L_BYTES];
  LAS unsigned char* L = (LAS unsigned char*)smem;
  const int tid = threadIdx.x, lane = tid & 63, w = __builtin_amdgcn_readfirstlane(tid >> 6);
  const int b = blockIdx.x, g = b & 7, j = b >> 3;
  const bool is_o = j < 28 && g < 4, is_f = j < 28 && g >= 4;
  const int jo = j >= 14 ? j - 14 : j, gs = j >= 14 ? g + 4 : (g + 1) & 3;
  const int f = (g - 4) * 28 + j, rp3 = (g - 4) * 2 + (j & 1);
  const int h = 2 * gs + (jo >= 7 ? 1 : 0), nc = jo >= 7 ? jo - 7 : jo;
  const int f0 = f * 16;
  const int m = lane & 15, kg = lane >> 4;
  const f32x4 z = {0.f, 0.f, 0.f, 0.f};
  const v4u sent4 = {SENT, SENT, SENT, SENT};
  STAMP(0);
  if (__builtin_expect(w >= 8, 0)) {
    int c0 = P2_OFF + (g * 32 + j) * P2S + lane * 16, c1 = lane < 8 ? P2_OFF + (g * 32 + j) * P2S + 1024 + lane * 16 : OOB;
    int c2_ = is_o ? P3_OFF + (lane >> 3) * P3R + h * P3S + nc * 128 + (lane & 7) * 16 : is_f ? P4_OFF + f * P4S + lane * 16 : OOB;
    int c3 = (is_f && lane < 56) ? P4_OFF + f * P4S + 1024 + lane * 16 : OOB;
    v4u sv4 = sent4;
    asm volatile("" : "+v"(c0), "+v"(c1), "+v"(c2_), "+v"(c3), "+v"(sv4));
    SCHED();
    ARGS();
    const u32 ep = ld32(rs, EPOCH_OFF);
#define PT_ALL() \
    u32 pt = 0; \
    if (lane < 8) { \
      const int hr = is_o ? h * 256 : 0, fr = is_f ? f0 : 0; \
      const u16* pa = (lane & 1) ? (const u16*)out : Vc + ((size_t)g * SK + (SK - WIN) + j * 64) * DH; \
      const u16* pb = wo + (size_t)(hr + (lane & 1) * 255) * D; \
      const u16* pc = w1 + (size_t)((lane & 1) * 447) * F + fr; \
      const u16* pd = (lane & 1) ? b1 + fr : w2 + (size_t)fr * D; \
      pt = ldp<u32>((lane & 4) ? ((lane & 2) ? pd : pc) : ((lane & 2) ? pb : pa)); \
    } \
    SCHED()
    st128(sv4, rs, c0); st128(sv4, rs, c1); st128(sv4, rs, c2_); st128(sv4, rs, c3);
    SCHED();
    BAR();
    asm volatile("s_waitcnt vmcnt(0)" ::: "memory");
    const u32 tag = __builtin_amdgcn_readfirstlane(ep) + 1u;
    st32(tag, rs, lane == 0 ? RDY_OFF + (g * 32 + j) * 4 : OOB);
    SCHED();
    STAMP(1);
    if (j >= 28) {
      const unsigned char* cp = (const unsigned char*)(pc0 & ~255ull) + (j - 28) * (CODE_BYTES / 4) + (lane < CODE_BYTES / 256 ? lane : 0) * 64;
      const u32 cv = ldp<u32>(cp);
      asm volatile("" :: "v"(cv));
    }
    if (is_o || is_f) {
      wait_flag(L, is_o ? 6 : 3, 1);
      for (int i = 0; i < RDELAY; ++i) __builtin_amdgcn_s_sleep(4);
      const int rdy_off = RDY_OFF + (is_o ? gs * 128 + (lane & 7) * 16 : lane * 16);
      for (u32 spins = 0;;) {
        CFENCE();
        const v4u r = ld128(rs, rdy_off);
        SPIN_END(__builtin_amdgcn_ballot_w64(!(r.x == tag && r.y == tag && r.z == tag && r.w == tag)) == 0);
        __builtin_amdgcn_s_sleep(8);
      }
      CFENCE();
      *(volatile LAS u32*)(L + L_FL + 16) = 1u;
      STAMP(5);
    }
    if (is_o) {
      __builtin_amdgcn_s_setprio(2);
      wait_flag(L, 7, 4);
      for (int i = 0; i < ODELAY; ++i) __builtin_amdgcn_s_sleep(1);
      const int s_off = P2_OFF + (gs * 32 + (lane & 31)) * P2S + 1024 + (h & 1) * 64;
      u32 sv = SENT;
      for (u32 spins = 0;;) {
        CFENCE();
        if (sv == SENT && lane < 32) sv = ld32(rs, s_off);
        SPIN_END((u32)__builtin_amdgcn_ballot_w64(sv == SENT) == 0);
      }
      STAMP(6);
      float M = asf(sv);
      M = fmaxf(M, dpp<DPP_XOR1>(M)); M = fmaxf(M, dpp<DPP_XOR2>(M)); M = fmaxf(M, dpp<DPP_ROR4>(M)); M = fmaxf(M, dpp<DPP_ROR8>(M));
      M = max_x16(M);
      const float e = __builtin_amdgcn_exp2f(asf(sv) - M);
      const float Ls = add_x16(row_sum16(e));
      if (lane < 32) *(volatile LAS u32*)(L + L_WT + lane * 4) = nosent(asu(e * __builtin_amdgcn_rcpf(Ls)));
      STAMP(7);
    }
    if (is_f) {
      __builtin_amdgcn_s_setprio(2);
      const int fo = (f >> 5) * 512 + (f & 1) * 256 + ((f >> 1) & 15) * 16;
      const int o0 = P4_OFF + lane * P4S + fo, o1 = lane < 48 ? P4_OFF + (64 + lane) * P4S + fo : OOB;
      BAR();
      const v2u hp = ld64(rs, P3_OFF + rp3 * P3R + m * P3S + f * 8);
      if (PDELAY) __builtin_amdgcn_s_sleep(PDELAY);
      v4u r0 = sent4, r1 = {hp.x << 16, hp.x & 0xFFFF0000u, hp.y << 16, hp.y & 0xFFFF0000u};
      GAS u16* op = (GAS u16*)out + f * 4 + kg;
      asm volatile("" : "+v"(op), "+v"(r1));
      bool ba = true, bb = lane < 48;
      u32 spins = 0;
      float ta, tb;
      do {
        SPIN_TOP();
        if (ba) r0 = ld128(rs, o0);
        if (bb) r1 = ld128(rs, o1);
        SCHED();
        ba = umax4(r0) > thr; bb = umax4(r1) > thr;
        SCHED();
        const f32x4 v = __builtin_bit_cast(f32x4, r0) + __builtin_bit_cast(f32x4, r1);
        auto t0 = __builtin_amdgcn_permlane32_swap(asu(v[0]), asu(v[2]), false, false);
        auto t1 = __builtin_amdgcn_permlane32_swap(asu(v[1]), asu(v[3]), false, false);
        ta = asf(t0[0]) + asf(t0[1]); tb = asf(t1[0]) + asf(t1[1]);
      } while (ANY2(ba, bb));
      STAMP(8);
      auto t2 = __builtin_amdgcn_permlane16_swap(asu(ta), asu(tb), false, false);
      const float u0 = row_sum16(asf(t2[0]) + asf(t2[1]));
      if (m == 0) {
        *op = (u16)pkbf(u0, u0);
        if (f == 0 && lane == 0) st32(tag, rs, EPOCH_OFF);
      }
      STAMP(9);
    }
    FIN();
  }
  if (__builtin_expect(w >= 4, 0)) {
    const int a = w - 4;
    if (a < 3) *(LAS v4u*)(L + (tid - 256) * 16) = tid < 416 ? sent4 : v4u{0u, 0u, 0u, 0u};
    u32 ko = ((g * SK + (SK - WIN) + j * 64 + a * 16 + m) * DH + kg * 8) * 2;
    u32 vo = ((g * SK + (SK - WIN) + j * 64 + (lane >> 3) * 8) * DH + a * 64 + (lane & 7) * 8) * 2;
    asm volatile("" : "+v"(ko), "+v"(vo));
    SCHED();
    ARGS();
    const u16* Kp = (const u16*)((const unsigned char*)Kc + ko);
    const u16* Vp = (const u16*)((const unsigned char*)Vc + vo);
    BAR();
    __builtin_amdgcn_s_setprio(1);
    v4u Kr[8], Vr[8];
#pragma unroll
    for (int i = 0; i < VPRE; ++i) Vr[i] = ldg<v4u>(Vp + i * DH);
    SCHED();
    if (a >= NKW) wait_flag(L, 5, 1);
    SCHED();
#pragma unroll
    for (int ks = 0; ks < 8; ++ks) Kr[ks] = ldg<v4u>(Kp + ks * 32);
    SCHED();
    if (a < NKW) wait_flag(L, 5, 1);
    SCHED();
#pragma unroll
    for (int i = VPRE; i < NV0; ++i) Vr[i] = ldg<v4u>(Vp + i * DH);
    SCHED();
    if (NV0 < 8) {
      wait_flag(L, 6, 4);
      SCHED();
#pragma unroll
      for (int i = NV0; i < 8; ++i) Vr[i] = ldg<v4u>(Vp + i * DH);
      SCHED();
    }
    bump_flag(L, 8, lane);
    STAMP(1);
    int qa_a = L_QS + (m & 7) * 528 + kg * 16, p_w = L_SC + (kg & 1) * 128 + (a * 16 + m) * 2, st_w = L_SC + 256 + (kg & 1) * 32 + a * 8;
    int pa_a = L_SC + (m >> 3) * 128 + (kg * 2 + ((m >> 2) & 1)) * 16, st_r = L_SC + 256 + (kg >> 1) * 32;
    int o_off = !(lane & 8) ? P2_OFF + (g * 32 + j) * P2S + (kg >> 1) * 512 + a * 128 + (kg & 1) * 64 + (lane & 7) * 8 : OOB;
    int l_off = (a == 0 && !(kg & 1)) ? P2_OFF + (g * 32 + j) * P2S + 1024 + (kg >> 1) * 64 + m * 4 : OOB;
    u32 keep = (m & 3) == kg ? 0xFFFFFFFFu : 0u;
    asm volatile("" : "+v"(qa_a), "+v"(p_w), "+v"(st_w), "+v"(pa_a), "+v"(st_r), "+v"(o_off), "+v"(l_off), "+v"(keep));
    for (u32 spins = 0;;) {
      CFENCE();
      const v4u fl4 = *(volatile LAS v4u*)(L + L_FL);
      SPIN_END(__builtin_amdgcn_readfirstlane(fl4.x & fl4.y & fl4.z & fl4.w) != 0);
    }
    CFENCE();
    v4u qa[8];
#pragma unroll
    for (int ks = 0; ks < 8; ++ks) qa[ks] = *(LAS v4u*)(L + qa_a + ks * 64);
    STAMP(2);
    f32x4 acc = z;
#pragma unroll
    for (int ks = 0; ks < 8; ++ks) acc = mfma(qa[ks], Kr[ks], acc);
    {
      const f32x2 a2 = acc.xy + acc.zw;
      float sv = a2.x + a2.y;
      sv = bflo(pkbf(sv, sv)) * (0.0625f * 1.4426950408889634f);
      float mw;
      asm volatile("s_nop 1\n\tv_max_f32_dpp %0, %1, %1 quad_perm:[1,0,3,2] row_mask:0xf bank_mask:0xf\n\t"
                   "s_nop 1\n\tv_max_f32_dpp %0, %0, %0 quad_perm:[2,3,0,1] row_mask:0xf bank_mask:0xf\n\t"
                   "s_nop 1\n\tv_max_f32_dpp %0, %0, %0 row_ror:4 row_mask:0xf bank_mask:0xf\n\t"
                   "s_nop 1\n\tv_max_f32_dpp %0, %0, %0 row_ror:8 row_mask:0xf bank_mask:0xf" : "=&v"(mw) : "v"(sv));
      const float p = __builtin_amdgcn_exp2f(sv - mw);
      *(volatile LAS u16*)(L + p_w) = (u16)(pkbf(p, p) & 0x7FFFu);
      const float lw = row_sum16(p);
      if (m == 0) *(volatile LAS v2u*)(L + st_w) = v2u{nosent(asu(mw)), nosent(asu(lw))};
    }
    STAMP(3);
    v4u B[8], pr, s0, s1;
#define P_READ() CFENCE(); pr = *(LAS v4u*)(L + pa_a); s0 = *(LAS v4u*)(L + st_r); s1 = *(LAS v4u*)(L + st_r + 16)
#pragma unroll
    for (int ii = 0; ii < 4; ++ii) {
      if (ii == PREAD) { P_READ(); SCHED(); }
#pragma unroll
      for (int jj = 0; jj < 8; ++jj)
        B[jj][ii] = (jj & 1) ? phi(Vr[2 * ii + 1][jj >> 1], Vr[2 * ii][jj >> 1]) : plo(Vr[2 * ii + 1][jj >> 1], Vr[2 * ii][jj >> 1]);
      SCHED();
    }
    asm volatile("" : "+v"(B[0]), "+v"(B[1]), "+v"(B[2]), "+v"(B[3]), "+v"(B[4]), "+v"(B[5]), "+v"(B[6]), "+v"(B[7]));
    STAMP(7);
    v4u pa;
    f32x4 c[8];
    {
      u32 spins = 0;
      bool bad;
      do {
        if (PREAD > 3 || spins) { P_READ(); }
        u32 mk = spins++ > SPIN_CAP ? 0u : 0x80008000u;
        asm volatile("" : "+s"(mk) :: "memory");
        SCHED();
        pa = pr & keep;
        c[0] = mfma(pa, B[0], z); SCHED();
        bad = (((pr.x | pr.y) | (pr.z | pr.w)) & mk) != 0; SCHED();
        c[4] = mfma(pa, B[4], z); SCHED();
      } while (__builtin_amdgcn_ballot_w64(bad) != 0);
    }
    float M, Ls;
    u32 lse;
    f32x4 fw;
#define STATS_A() M = fmaxf(fmaxf(asf(s0.x), asf(s0.z)), fmaxf(asf(s1.x), asf(s1.z))); \
    fw = f32x4{__builtin_amdgcn_exp2f(asf(s0.x) - M), __builtin_amdgcn_exp2f(asf(s0.z) - M), __builtin_amdgcn_exp2f(asf(s1.x) - M), __builtin_amdgcn_exp2f(asf(s1.z) - M)}
#define STATS_B() Ls = (fw[0] * asf(s0.y) + fw[1] * asf(s0.w)) + (fw[2] * asf(s1.y) + fw[3] * asf(s1.w)); fw *= __builtin_amdgcn_rcpf(Ls); \
    lse = nosent(asu(M + __builtin_amdgcn_logf(Ls)))
    c[1] = mfma(pa, B[1], z); SCHED();
    const bool sbad = umax2(umax4(s0), umax4(s1)) == SENT; SCHED();
    c[5] = mfma(pa, B[5], z); SCHED();
    STATS_A(); asm volatile("" : "+v"(fw), "+v"(M)); SCHED();
    c[2] = mfma(pa, B[2], z); c[6] = mfma(pa, B[6], z); SCHED();
    STATS_B(); asm volatile("" : "+v"(fw), "+v"(lse)); SCHED();
    c[3] = mfma(pa, B[3], z); c[7] = mfma(pa, B[7], z); SCHED();
    if (__builtin_expect(__builtin_amdgcn_ballot_w64(sbad) != 0, 0)) {
      for (u32 spins = 0;;) {
        CFENCE();
        s0 = *(LAS v4u*)(L + st_r); s1 = *(LAS v4u*)(L + st_r + 16);
        SPIN_END(__builtin_amdgcn_ballot_w64(umax2(umax4(s0), umax4(s1)) == SENT) == 0);
      }
      STATS_A(); STATS_B();
    }
    st32(lse, rs, l_off);
    STAMP(4);
    float os[4];
#pragma unroll
    for (int jj = 0; jj < 4; ++jj) {
      const f32x2 oa = __builtin_elementwise_fma(fw.zw, c[jj].zw, fw.xy * c[jj].xy), ob = __builtin_elementwise_fma(fw.zw, c[jj + 4].zw, fw.xy * c[jj + 4].xy);
      float o0 = oa.x + oa.y, o1 = ob.x + ob.y;
      asm volatile("" : "+v"(o0), "+v"(o1));
      auto r = __builtin_amdgcn_permlane16_swap(asu(o0), asu(o1), false, false);
      os[jj] = asf(r[0]) + dpp<DPP_ROR8>(asf(r[1]));
      asm volatile("" : "+v"(os[jj]));
    }
    {
      v2u ov; ov.x = nosent(pkbf(os[0], os[1])); ov.y = nosent(pkbf(os[2], os[3]));
      st64(ov, rs, o_off);
    }
    SCHED();
    bump_flag(L, 7, lane);
    STAMP(5);
    FIN();
  }
  {
    __builtin_amdgcn_s_setprio(3);
    const int part = j & 3, cb = j >> 2;
    const int qr = tid >> 3, qrow = part * 112 + (qr < 28 ? qr : 27) * 4;
    u32 xo = (lane < 56 ? lane : 55) * 16, xo2 = qrow * 2, wo_[4];
#pragma unroll
    for (int i = 0; i < 4; ++i) wo_[i] = ((qrow + i) * NQ + g * 512 + cb * 64 + (lane & 7) * 8) * 2;
    asm volatile("" : "+v"(xo), "+v"(xo2), "+v"(wo_[0]), "+v"(wo_[1]), "+v"(wo_[2]), "+v"(wo_[3]));
    SCHED();
    ARGS();
    const v4u xa = ldp<v4u>((const unsigned char*)x + xo);
    const v2u xq = ldp<v2u>((const unsigned char*)x + xo2);
    SCHED();
    v4u Wr[4];
#pragma unroll
    for (int i = 0; i < 4; ++i) Wr[i] = ldx<v4u, NT_LOADS>((const u16*)((const unsigned char*)wq + wo_[i]));
    SCHED();
    const u32 ep = ld32(rs, w == 0 ? EPOCH_OFF : OOB);
    SCHED();
    BAR();
    int qp_w = L_QP + w * 256 + (lane & 7) * 32 + kg * 8, qp_r = L_QP + (lane & 31) * 8;
    int pub_off = lane < 32 ? Q1_OFF + g * 8192 + part * 2048 + cb * 256 + lane * 8 : OOB;
    int q_off = Q1_OFF + g * 8192 + w * 2048 + lane * 32, qs_w = L_QS + ((lane >> 5) * 4 + w) * 528 + (lane & 31) * 16;
    float a[8] = {0.f, 0.f, 0.f, 0.f, 0.f, 0.f, 0.f, 0.f};
    asm volatile("" : "+v"(qp_w), "+v"(qp_r), "+v"(pub_off), "+v"(q_off), "+v"(qs_w), "+v"(a[0]), "+v"(a[1]), "+v"(a[2]), "+v"(a[3]), "+v"(a[4]), "+v"(a[5]), "+v"(a[6]), "+v"(a[7]));
    SCHED();
    const float piv = bflo(__builtin_amdgcn_readfirstlane(xa[0]));
    f32x2 d1 = {0.f, 0.f}, d2 = {0.f, 0.f};
#pragma unroll
    for (int i = 0; i < 4; ++i) { const f32x2 d = f32x2{bflo(xa[i]), bfhi(xa[i])} - piv; d1 += d; d2 = __builtin_elementwise_fma(d, d, d2); }
    const float s1 = lane < 56 ? d1.x + d1.y : 0.f, s2 = lane < 56 ? d2.x + d2.y : 0.f;
    auto sr = __builtin_amdgcn_permlane32_swap(asu(s1), asu(s2), false, false);
    const float st = add_x16(row_sum16(asf(sr[0]) + asf(sr[1]))) * (1.0f / D);
    const float md = asf(__builtin_amdgcn_readlane(asu(st), 0)), mq = asf(__builtin_amdgcn_readlane(asu(st), 32));
    const float mu = piv + md;
    float rstd = __builtin_amdgcn_rsqf(fmaxf(mq - md * md, 0.f) + 1e-5f);
    rstd = qr < 28 ? rstd : 0.f;
    const u32 xn0 = pkbf((bflo(xq.x) - mu) * rstd, (bfhi(xq.x) - mu) * rstd);
    const u32 xn1 = pkbf((bflo(xq.y) - mu) * rstd, (bfhi(xq.y) - mu) * rstd);
    STAMP(11);
#pragma unroll
    for (int jj = 0; jj < 4; ++jj) {
      a[2 * jj] = dot2(plo(Wr[1][jj], Wr[0][jj]), xn0, a[2 * jj]);
      a[2 * jj + 1] = dot2(phi(Wr[1][jj], Wr[0][jj]), xn0, a[2 * jj + 1]);
    }
    SCHED();
#pragma unroll
    for (int jj = 0; jj < 4; ++jj) {
      const int k = (jj >> 1) | ((jj & 1) << 1);
      a[2 * k] = dot2(plo(Wr[3][k], Wr[2][k]), xn1, a[2 * k]);
      a[2 * k + 1] = dot2(phi(Wr[3][k], Wr[2][k]), xn1, a[2 * k + 1]);
    }
    float q2[2];
    {
      float t[4];
#pragma unroll
      for (int jj = 0; jj < 4; ++jj) {
        auto r = __builtin_amdgcn_permlane32_swap(asu(a[jj]), asu(a[jj + 4]), false, false);
        t[jj] = asf(r[0]) + asf(r[1]);
      }
#pragma unroll
      for (int jj = 0; jj < 2; ++jj) {
        auto r = __builtin_amdgcn_permlane16_swap(asu(t[jj]), asu(t[jj + 2]), false, false);
        q2[jj] = asf(r[0]) + asf(r[1]);
        asm volatile("" : "+v"(q2[jj]));
        q2[jj] += dpp<DPP_ROR8>(q2[jj]);
        asm volatile("" : "+v"(q2[jj]));
      }
    }
    *(LAS v2u*)(L + qp_w) = v2u{nosent(asu(q2[0])), nosent(asu(q2[1]))};
    SCHED();
    u32 tag;
    STAMP(1);
    if (w == 0) {
      tag = __builtin_amdgcn_readfirstlane(ep) + 1u;
      *(volatile LAS u32*)(L + L_FL + 36) = tag;
      u32 spins = 0;
      bool bad;
      v2u gr; gr.y = tag;
      do {
        SPIN_TOP();
        const v2u p0 = *(LAS v2u*)(L + qp_r), p1 = *(LAS v2u*)(L + qp_r + 256), p2 = *(LAS v2u*)(L + qp_r + 512), p3 = *(LAS v2u*)(L + qp_r + 768);
        bad = umax2(umax4(v4u{p0.x, p0.y, p1.x, p1.y}), umax4(v4u{p2.x, p2.y, p3.x, p3.y})) > thr;
        const f32x2 qs = (__builtin_bit_cast(f32x2, p0) + __builtin_bit_cast(f32x2, p1)) + (__builtin_bit_cast(f32x2, p2) + __builtin_bit_cast(f32x2, p3));
        gr.x = pkbf(qs.x, qs.y);
        asm volatile("" : "+v"(gr));
      } while (__builtin_amdgcn_ballot_w64(bad) != 0);
      st64(gr, rs, pub_off);
      SCHED();
      *(volatile LAS u32*)(L + L_FL + 20) = 1u;
      STAMP(2);
    }
    if (w != 0) {
      wait_flag(L, 5, 1);
      CFENCE();
      tag = __builtin_amdgcn_readfirstlane(*(volatile LAS u32*)(L + L_FL + 36));
    }
    for (int i = 0; i < QDELAY; ++i) __builtin_amdgcn_s_sleep(4);
#define Q_ISSUE() v4u g0 = ld128(rs, q_off), g1 = ld128(rs, q_off + 16); SCHED(); bump_flag(L, 6, lane); STAMP(9)
#define QCNT()
#define Q_FINISH() \
    { \
      u32 spins = 0; \
      bool bad = false; \
      do { \
        u32 mk;     \
        asm volatile("s_cmp_gt_u32 %1, %2\n\ts_cselect_b32 %0, 0, -1" : "=s"(mk) : "s"(spins++), "s"(SPIN_CAP) : "scc", "memory"); \
        if (bad) { g0 = ld128(rs, q_off); g1 = ld128(rs, q_off + 16); } \
        *(LAS v4u*)(L + qs_w) = v4u{g0.x, g0.z, g1.x, g1.z};     \
        bad = ((((g0.y ^ tag) | (g0.w ^ tag)) | ((g1.y ^ tag) | (g1.w ^ tag))) & mk) != 0; \
      } while (__builtin_amdgcn_ballot_w64(bad) != 0); \
    } \
    CFENCE(); \
    *(volatile LAS u32*)(L + L_FL + w * 4) = 1u; \
    __builtin_amdgcn_s_setprio(0); \
    STAMP(3)
    if (is_o) {
      Q_ISSUE();
      Q_FINISH();
      SCHED();
      u32 xv = ldp<u32>(x + nc * 64 + (lane & 31) * 2);
      SCHED();
      v2u r[16];
      const u16* p = wo + (size_t)(h * 256 + w * 64 + kg * 16) * D + nc * 64 + m * 4;
#pragma unroll
      for (int i = 0; i < 16; ++i) r[i] = ldx<v2u, NT_TAIL>(p + (((i >> 2) & 1) * 8 + (i >> 3) * 4 + (i & 3)) * D);
      SCHED();
      wait_flag(L, 4, 1);
      wait_flag(L, 7, 4);
      for (int i = 0; i < ODELAY; ++i) __builtin_amdgcn_s_sleep(1);
      STAMP(5);
      __builtin_amdgcn_s_setprio(2);
      const int a_off = P2_OFF + (gs * 32 + m) * P2S + (h & 1) * 512 + w * 128 + kg * 16;
      v4u A[4];
      A[0] = ld128(rs, a_off); A[1] = ld128(rs, a_off + 64); A[2] = ld128(rs, a_off + 16 * P2S); A[3] = ld128(rs, a_off + 16 * P2S + 64);
      SCHED();
      xv = h == 0 ? xv : 0u;
      v4u B[2][4];
#pragma unroll
      for (int s = 0; s < 2; ++s)
#pragma unroll
        for (int ii = 0; ii < 4; ++ii) {
          const v2u lo = r[s * 8 + 2 * ii], hi = r[s * 8 + 2 * ii + 1];
          B[s][0][ii] = plo(hi.x, lo.x); B[s][1][ii] = phi(hi.x, lo.x); B[s][2][ii] = plo(hi.y, lo.y); B[s][3][ii] = phi(hi.y, lo.y);
        }
      asm volatile("" : "+v"(xv), "+v"(B[0][0]), "+v"(B[0][1]), "+v"(B[0][2]), "+v"(B[0][3]), "+v"(B[1][0]), "+v"(B[1][1]), "+v"(B[1][2]), "+v"(B[1][3]));
      int wt_a = L_WT + kg * 16, red_w = L_RED + w * 256 + m * 16 + kg * 4, red_r = L_RED + (lane & 31) * 8;
      int p3_off = P3_OFF + (2 * w + (lane >> 5)) * P3R + h * P3S + nc * 128 + (lane & 31) * 4;
      f32x2 xr = {bflo(xv), bfhi(xv)};
      asm volatile("" : "+v"(wt_a), "+v"(red_w), "+v"(red_r), "+v"(p3_off), "+v"(xr));
      STAMP(4);
      f32x4 C0[4], C1[4];
      bool ba = false, bb = false;
      u32 spins = 0;
      do {
        SPIN_TOP();
        if (ba) { A[0] = ld128(rs, a_off); A[1] = ld128(rs, a_off + 64); }
        if (bb) { A[2] = ld128(rs, a_off + 16 * P2S); A[3] = ld128(rs, a_off + 16 * P2S + 64); }
        SCHED();
        C0[0] = mfma(A[0], B[0][0], z); SCHED();
        const u32 ma = umax4(A[0]); SCHED();
        C0[1] = mfma(A[0], B[0][1], z); SCHED();
        ba = umax2(ma, umax4(A[1])) > thr; SCHED();
        C0[0] = mfma(A[1], B[1][0], C0[0]); SCHED();
        const u32 mb = umax4(A[2]); SCHED();
        C1[0] = mfma(A[2], B[0][0], z); SCHED();
        bb = umax2(mb, umax4(A[3])) > thr; SCHED();
        C0[1] = mfma(A[1], B[1][1], C0[1]); SCHED();
        C1[0] = mfma(A[3], B[1][0], C1[0]); SCHED();
      } while (ANY2(ba, bb));
      STAMP(7);
      C1[1] = mfma(A[2], B[0][1], z); C0[2] = mfma(A[0], B[0][2], z); SCHED();
      CFENCE();
      v4u e0 = *(LAS v4u*)(L + wt_a), e1 = *(LAS v4u*)(L + wt_a + 64);
      SCHED();
      C1[1] = mfma(A[3], B[1][1], C1[1]); C1[2] = mfma(A[2], B[0][2], z); C0[2] = mfma(A[1], B[1][2], C0[2]); C0[3] = mfma(A[0], B[0][3], z); SCHED();
      C1[2] = mfma(A[3], B[1][2], C1[2]); C1[3] = mfma(A[2], B[0][3], z); SCHED();
#define COMB(jj) { \
        const f32x4 ea = __builtin_bit_cast(f32x4, e0), eb = __builtin_bit_cast(f32x4, e1); \
        f32x2 c = ea.xy * C0[jj].xy, d = eb.xy * C1[jj].xy; \
        c = __builtin_elementwise_fma(ea.zw, C0[jj].zw, c); d = __builtin_elementwise_fma(eb.zw, C1[jj].zw, d); \
        c += d; t[jj] = c.x + c.y; }
      float t[4];
      const bool be = umax2(umax4(e0), umax4(e1)) == SENT; SCHED();
      C0[3] = mfma(A[1], B[1][3], C0[3]); SCHED();
      COMB(0); asm volatile("" : "+v"(t[0])); SCHED();
      C1[3] = mfma(A[3], B[1][3], C1[3]); SCHED();
      COMB(2); COMB(1); asm volatile("" : "+v"(t[1]), "+v"(t[2])); SCHED();
      if (__builtin_expect(__builtin_amdgcn_ballot_w64(be) != 0, 0)) {
        for (u32 spins = 0;;) {
          CFENCE();
          e0 = *(LAS v4u*)(L + wt_a); e1 = *(LAS v4u*)(L + wt_a + 64);
          SPIN_END(__builtin_amdgcn_ballot_w64(umax2(umax4(e0), umax4(e1)) == SENT) == 0);
        }
        COMB(0); COMB(2); COMB(1);
      }
      auto r0 = __builtin_amdgcn_permlane32_swap(asu(t[0]), asu(t[2]), false, false);
      const float ra = asf(r0[0]) + asf(r0[1]);
      COMB(3);
      auto r1 = __builtin_amdgcn_permlane32_swap(asu(t[1]), asu(t[3]), false, false);
      auto r2 = __builtin_amdgcn_permlane16_swap(asu(ra), asu(asf(r1[0]) + asf(r1[1])), false, false);
      *(LAS float*)(L + red_w) = asf(r2[0]) + asf(r2[1]);
      BAR();
      const LAS f32x2* rp = (const LAS f32x2*)(L + red_r);
      const f32x2 s = ((rp[0] + rp[32]) + xr) + (rp[64] + rp[96]);
      st32(nosent(pkbf(s[0], s[1])), rs, p3_off);
      STAMP(8);
    } else {
      Q_ISSUE();
      Q_FINISH();
    }
    if (is_f) {
      const int c2 = w * 128 + m * 8, ci = kg * 4 + (m & 3);
      v4u W2r[4];
      u32 b1r;
#define W2_LOAD() { \
        b1r = ldp<u16>(b1 + f0 + ci); \
        const u16* p2 = w2 + (size_t)(f0 + kg * 4) * D + (c2 < D ? c2 : 0); \
        _Pragma("unroll") for (int i = 0; i < 4; ++i) W2r[i] = ldx<v4u, NT_TAIL>(p2 + i * D); \
        SCHED(); }
      W2_LOAD();
      if (TAIL_GATE & 2) wait_flag(L, 7, 4);
      for (int i = 0; i < FDELAY; ++i) __builtin_amdgcn_s_sleep(4);
      SCHED();
      const int c = tid & 1, rq = tid >> 1, rqc = rq < 112 ? rq : 111;
      v4u W1r[4];
      {
        const u16* p = w1 + (size_t)(rqc * 4) * F + f0 + c * 8;
#pragma unroll
        for (int i = 0; i < 4; ++i) W1r[i] = ldx<v4u, NT_WQ>(p + ((i + ROT * f) & 3) * F);
        SCHED();
      }
      v2u B2[8];
#pragma unroll
      for (int jj = 0; jj < 8; ++jj) {
        B2[jj].x = (jj & 1) ? phi(W2r[1][jj >> 1], W2r[0][jj >> 1]) : plo(W2r[1][jj >> 1], W2r[0][jj >> 1]);
        B2[jj].y = (jj & 1) ? phi(W2r[3][jj >> 1], W2r[2][jj >> 1]) : plo(W2r[3][jj >> 1], W2r[2][jj >> 1]);
        asm volatile("" : "+v"(B2[jj]));
      }
      float b1v = bflo(b1r);
#pragma unroll
      for (int i = 0; i < 4; ++i) *(LAS v4u*)(L + L_W1 + (rq * 4 + ((i + ROT * f) & 3)) * 32 + c * 16) = rq < 112 ? W1r[i] : v4u{0u, 0u, 0u, 0u};
      LDSW();
      v4u Bf[4];
#pragma unroll
      for (int s = 0; s < 4; ++s) {
        LAS unsigned char* tp = L + L_W1 + ((4 * w + s) * 32 + kg * 8 + (m >> 2)) * 32 + (m & 3) * 8;
        const v2u lo = __builtin_bit_cast(v2u, __builtin_amdgcn_ds_read_tr16_b64_v4i16((LAS s16x4*)tp));
        const v2u hi = __builtin_bit_cast(v2u, __builtin_amdgcn_ds_read_tr16_b64_v4i16((LAS s16x4*)(tp + 128)));
        Bf[s] = v4u{lo.x, lo.y, hi.x, hi.y};
      }
      const v4u ones = {0x3F803F80u, 0x3F803F80u, 0x3F803F80u, 0x3F803F80u};
      f32x4 cs = z;
#pragma unroll
      for (int s = 0; s < 4; ++s) cs = mfma(ones, Bf[s], cs);
      float csv = cs[0] * 0.25f;
      asm volatile("" : "+v"(csv), "+v"(b1v));
      STAMP(4);
      __builtin_amdgcn_s_setprio(2);
      wait_flag(L, 4, 1);
      STAMP(5);
      const int a_off = P3_OFF + rp3 * P3R + m * P3S + w * 256 + kg * 16;
      const int a_off2 = w < 3 ? a_off + 128 : OOB;
      int red_w = L_RED + w * 256 + m * 16 + kg * 4, red_r = L_RED + ci * 16;
      int p4_off = (kg < 2 && c2 < D) ? P4_OFF + f * P4S + w * 512 + kg * 256 + m * 16 : OOB;
      v4u onev = ones;
      asm volatile("" : "+v"(red_w), "+v"(red_r), "+v"(p4_off), "+v"(onev));
      v4u A[4] = {sent4, sent4, sent4, sent4};
      bool ba = true, bb = true;
      f32x4 U, G, S;
      u32 spins = 0;
      do {
        SPIN_TOP();
        if (!SELP || ba) { A[0] = ld128(rs, a_off); A[1] = ld128(rs, a_off + 64); }
        if (!SELP || bb) { A[2] = ld128(rs, a_off2); A[3] = ld128(rs, a_off2 + 64); }
        SCHED();
        G = mfma(A[0], A[0], z); SCHED();
        const u32 ma = umax4(A[0]); SCHED();
        G = mfma(A[1], A[1], G); SCHED();
        ba = umax2(ma, umax4(A[1])) > thr; SCHED();
        G = mfma(A[2], A[2], G); SCHED();
        const u32 mb = umax4(A[2]); SCHED();
        G = mfma(A[3], A[3], G); SCHED();
        bb = umax2(mb, umax4(A[3])) > thr; SCHED();
        U = mfma(A[0], Bf[0], z); S = mfma(A[0], onev, z); SCHED();
      } while (ANY2(ba, bb));
      STAMP(7);
      U = mfma(A[1], Bf[1], U); SCHED();
      float gq = (G[0] + G[1]) + (G[2] + G[3]); SCHED();
      S = mfma(A[1], onev, S); SCHED();
      gq += dpp<DPP_XOR1>(gq); gq += dpp<DPP_XOR2>(gq); SCHED();
      U = mfma(A[2], Bf[2], U); SCHED();
      gq += dpp<DPP_ROR4>(gq); gq += dpp<DPP_ROR8>(gq); SCHED();
      S = mfma(A[2], onev, S); SCHED();
      auto r1 = __builtin_amdgcn_permlane32_swap(asu(csv), asu(gq), false, false); SCHED();
      U = mfma(A[3], Bf[3], U); SCHED();
      const float rb = asf(r1[0]) + asf(r1[1]); SCHED();
      S = mfma(A[3], onev, S); SCHED();
      const f32x2 u2 = U.xy + U.zw, s2 = S.xy + S.zw;
      auto r0 = __builtin_amdgcn_permlane32_swap(asu(u2.x + u2.y), asu(s2.x + s2.y), false, false);
      auto r2 = __builtin_amdgcn_permlane16_swap(asu(asf(r0[0]) + asf(r0[1])), asu(rb), false, false);
      *(LAS float*)(L + red_w) = asf(r2[0]) + asf(r2[1]);
      BAR();
      const LAS f32x4* rp = (const LAS f32x4*)(L + red_r);
      const f32x4 t = (rp[0] + rp[16]) + (rp[32] + rp[48]);
      const float mu2 = t[2] * (1.0f / D);
      const float var = fmaxf(t[3] * (1.0f / D) - mu2 * mu2, 0.f);
      const float rstd2 = __builtin_amdgcn_rsqf(var + 1e-5f);
      const float u = (t[0] - mu2 * t[1]) * rstd2 + b1v;
      const float n = __builtin_amdgcn_exp2f(-fabsf(u) * 1.4426950408889634f);
      const float num = u > 0.f ? 1.f + 2.f * n : n * n + 2.f * n;
      const float den = u > 0.f ? num + 2.f * n * n : num + 2.f;
      const float act = u * num * __builtin_amdgcn_rcpf(den);
      const u32 ap = pkbf(act, dpp<DPP_XOR1>(act));
      v2u a2; a2.x = asu(dpp<0x00>(asf(ap))); a2.y = asu(dpp<0xAA>(asf(ap)));
      f32x4 o2[8];
#pragma unroll
      for (int jj = 0; jj < 8; ++jj)
        o2[jj] = __builtin_amdgcn_mfma_f32_16x16x16bf16_1k(__builtin_bit_cast(s16x4, a2), __builtin_bit_cast(s16x4, B2[jj]), z, 0, 0, 0);
      v4u ov;
#pragma unroll
      for (int i = 0; i < 4; ++i) ov[i] = nosent(asu((kg & 1) ? o2[4 + i][0] : o2[i][0]));
      st128(ov, rs, p4_off);
      STAMP(8);
    }
    FIN();
  }
}

extern "C" void kernel_launch(void* const* d_in, const int* in_sizes, int n_in, void* d_out, int out_size, void* d_ws,
                              size_t ws_size, hipStream_t stream) {
  fused_block<<<NWG, NTHR, 0, stream>>>((const u16*)d_in[0], (const u16*)d_in[1], (const u16*)d_in[2], (const u16*)d_in[3],
                                        (const u16*)d_in[4], (const u16*)d_in[6], (const u16*)d_in[7], (const u16*)d_in[8],
                                        (u16*)d_out, (unsigned char*)d_ws);
}
```
